```python
import torch

def _softmax(x, axis=-1):
    return torch.softmax(x, dim=axis)
def _gelu(x):
    return torch.nn.functional.gelu(x, approximate="tanh")
def _silu(x):
    return torch.nn.functional.silu(x)
def _sigmoid(x):
    return torch.sigmoid(x)
def _layernorm(x, axis=-1, eps=1e-5):
    m = x.mean(dim=axis, keepdim=True)
    v = x.var(dim=axis, keepdim=True, unbiased=False)
    return (x - m) / torch.sqrt(v + eps)
def _scatter_add(tgt, idx, src, axis=0):
    return tgt.clone().index_add_(axis, idx, src)
def _segment_sum(src, seg, n):
    out = torch.zeros((n,) + src.shape[1:], dtype=src.dtype)
    return out.index_add_(0, seg, src)
def _pad(x, pw):
    flat = tuple(v for pair in reversed(pw) for v in pair)
    return torch.nn.functional.pad(x, flat)

kernel_name = 'sequential_preLN__sliding_block_cached__dense_activation_gelu__decode'

def setup_inputs():

    _w = (lambda a: (lambda t: t.to(torch.bfloat16) if t.is_floating_point() else t)(torch.as_tensor(a)))
    def _A_setup():
        B, Sq, D = 1, 1, 512
        Hq, Hkv, Sk, Dh = 32, 8, 65536, 128
        assert Hq % Hkv == 0, (Hq, Hkv)
        return {
            "x": _w((lambda *s: torch.randn(*s, dtype=torch.bfloat16) * 0.1)(B, Sq, D)),
            "k_cache": _w((lambda *s: torch.randn(*s, dtype=torch.bfloat16) * 1.6)(B, Hkv, Sk, Dh)),
            "v_cache": _w((lambda *s: torch.randn(*s, dtype=torch.bfloat16) * 1.6)(B, Hkv, Sk, Dh)),
            "wq": _w((lambda *s: torch.randn(*s, dtype=torch.bfloat16) * 1.6)(D, Hq * Dh) * (D ** -0.5)),
            "wo": _w((lambda *s: torch.randn(*s, dtype=torch.bfloat16) * 1.6)(Hq * Dh, D) * ((Hq * Dh) ** -0.5)),
        }
    _A_in = _A_setup()

    def _B_setup():
        B, S, D, F = 1, 1, 512, 1792
        return {
            "x": _w((lambda *s: torch.randn(*s, dtype=torch.bfloat16) * 0.1)(B, S, D)),
            "w1": _w((lambda *s: torch.randn(*s, dtype=torch.bfloat16) * 3.2)(D, F) * (D ** -0.5)),
            "b1": _w((lambda *s: torch.randn(*s, dtype=torch.bfloat16) * 3.2)(F) * 0.01),
            "w2": _w((lambda *s: torch.randn(*s, dtype=torch.bfloat16) * 3.2)(F, D) * (F ** -0.5)),
            "b2": _w((lambda *s: torch.randn(*s, dtype=torch.bfloat16) * 3.2)(D) * 0.01),
        }
    _B_in = _B_setup()

    return {
        "A_x": _A_in["x"],
        "A_k_cache": _A_in["k_cache"],
        "A_v_cache": _A_in["v_cache"],
        "A_wq": _A_in["wq"],
        "A_wo": _A_in["wo"],
        "B_x": _B_in["x"],
        "B_w1": _B_in["w1"],
        "B_b1": _B_in["b1"],
        "B_w2": _B_in["w2"],
        "B_b2": _B_in["b2"],
    }

def reference(A_x, A_k_cache, A_v_cache, A_wq, A_wo, B_x, B_w1, B_b1, B_w2, B_b2):
    def _A_ref(x, k_cache, v_cache, wq, wo):
        B, Sq, D = x.shape
        Hq, Dh = 32, 128
        Hkv, Sk = k_cache.shape[1], k_cache.shape[2]
        W = min(4096, max(256, Sk // 4))
        rep = Hq // Hkv
        q = (lambda x, *d: x.permute(*d))((x @ wq).reshape(B, Sq, Hq, Dh), 0, 2, 1, 3)
        k = (lambda x, n, axis: torch.repeat_interleave(x, n, dim=axis))(k_cache[:, :, Sk - W:, :], rep, axis=1)
        v = (lambda x, n, axis: torch.repeat_interleave(x, n, dim=axis))(v_cache[:, :, Sk - W:, :], rep, axis=1)
        scores = torch.einsum("bhqd,bhkd->bhqk", q, k) * (Dh ** -0.5)
        p = _softmax(scores, axis=-1)
        o = torch.einsum("bhqk,bhkd->bhqd", p, v)
        return (lambda x, *d: x.permute(*d))(o, 0, 2, 1, 3).reshape(B, Sq, Hq * Dh) @ wo

    def _B_ref(x, w1, b1, w2, b2):
        h = torch.einsum("bsd,df->bsf", x, w1) + b1
        h = _gelu(h)
        return torch.einsum("bsf,fd->bsd", h, w2) + b2

    x = A_x
    h = x + _A_ref(_layernorm(x, axis=-1), A_k_cache, A_v_cache, A_wq, A_wo)
    h2 = h + _B_ref(_layernorm(h, axis=-1), B_w1, B_b1, B_w2, B_b2)
    return h2
```

```cpp
#include <hip/hip_runtime.h>
#include <hip/hip_bf16.h>
#include <stdint.h>

typedef unsigned long long u64;
typedef __attribute__((ext_vector_type(4))) unsigned u32x4;
typedef __attribute__((ext_vector_type(2))) unsigned u32x2;
typedef __attribute__((ext_vector_type(4))) float f32x4;
typedef __attribute__((ext_vector_type(2))) float f32x2;
typedef __attribute__((ext_vector_type(8))) short s16x8;

constexpr int DM = 512, HQ = 32, HKV = 8, SK = 65536, DH = 128, WIN = 4096, FF = 1792;
constexpr int NBLK = 256, NTHR = 768;
constexpr int NMLP = 56, NFIN = 8;

constexpr size_t WS_EPOCH = 0;
constexpr size_t WS_H1 = 4096;
constexpr size_t WS_H2O = WS_H1 + (size_t)8 * 4 * 512 * 8;
constexpr size_t WS_H2S = WS_H2O + (size_t)8 * 32 * 512 * 8;
constexpr size_t WS_H3 = WS_H2S + (size_t)8 * 32 * 8 * 8;
constexpr size_t WS_H4 = WS_H3 + (size_t)16 * 512 * 8;
constexpr size_t WS_END = WS_H4 + (size_t)NMLP * 512 * 8;

constexpr unsigned SPIN_CAP = 1u << 18;

#define KV_NT 1

__device__ __forceinline__ float bf16r(float x) { return __bfloat162float(__float2bfloat16(x)); }
__device__ __forceinline__ float bflo(unsigned u) { return __uint_as_float(u << 16); }
__device__ __forceinline__ float bfhi(unsigned u) { return __uint_as_float(u & 0xffff0000u); }

template <int CTRL> __device__ __forceinline__ float dpp(float x) {
  return __builtin_bit_cast(float, __builtin_amdgcn_mov_dpp(__builtin_bit_cast(int, x), CTRL, 0xf, 0xf, true));
}
constexpr int XOR1 = 0xB1, XOR2 = 0x4E, ROR4 = 0x124, ROR8 = 0x128;
__device__ __forceinline__ float sw16_sum(float x) {
  auto s = __builtin_amdgcn_permlane16_swap(__float_as_uint(x), __float_as_uint(x), false, false);
  return __uint_as_float(s[0]) + __uint_as_float(s[1]);
}
__device__ __forceinline__ float sw32_sum(float x) {
  auto s = __builtin_amdgcn_permlane32_swap(__float_as_uint(x), __float_as_uint(x), false, false);
  return __uint_as_float(s[0]) + __uint_as_float(s[1]);
}
__device__ __forceinline__ float sw16_max(float x) {
  auto s = __builtin_amdgcn_permlane16_swap(__float_as_uint(x), __float_as_uint(x), false, false);
  return fmaxf(__uint_as_float(s[0]), __uint_as_float(s[1]));
}
__device__ __forceinline__ float row16_sum(float x) {
  x += dpp<XOR1>(x); x += dpp<XOR2>(x); x += dpp<ROR4>(x); x += dpp<ROR8>(x); return x;
}
__device__ __forceinline__ float row16_max(float x) {
  x = fmaxf(x, dpp<XOR1>(x)); x = fmaxf(x, dpp<XOR2>(x)); x = fmaxf(x, dpp<ROR4>(x)); x = fmaxf(x, dpp<ROR8>(x)); return x;
}
__device__ __forceinline__ float wave_sum(float x) { return sw32_sum(sw16_sum(row16_sum(x))); }

__device__ __forceinline__ u32x4 ldw16(const void* p) {
  return __builtin_nontemporal_load(reinterpret_cast<const u32x4*>(p));
}
__device__ __forceinline__ unsigned ldw4(const void* p) {
  return __builtin_nontemporal_load(reinterpret_cast<const unsigned*>(p));
}

struct Tag { unsigned hi, lo; };
__device__ __forceinline__ void put(u64* g, float v, Tag t) {
  const unsigned lo = (__float_as_uint(v) & 0xffffff00u) | t.lo;
  __hip_atomic_store(g, ((u64)t.hi << 32) | lo, __ATOMIC_RELAXED, __HIP_MEMORY_SCOPE_AGENT);
}
__device__ __forceinline__ u64 get(const u64* g) {
  return __hip_atomic_load(g, __ATOMIC_RELAXED, __HIP_MEMORY_SCOPE_AGENT);
}
__device__ __forceinline__ bool gok(u64 x, Tag t) { return ((unsigned)(x >> 32) == t.hi) & (((unsigned)x & 0xffu) == t.lo); }
__device__ __forceinline__ float gval(u64 x) { return __uint_as_float((unsigned)x & 0xffffff00u); }

__device__ __forceinline__ void bar() {
  asm volatile("s_waitcnt lgkmcnt(0)" ::: "memory");
  __builtin_amdgcn_s_barrier();
  asm volatile("" ::: "memory");
}
__device__ __forceinline__ void lds_fence() { asm volatile("s_waitcnt lgkmcnt(0)" ::: "memory"); }

#define STAMP(k) do { } while (0)

struct Lds {
  float xs[4][512];
  float qpart[4][64];
  unsigned qs[4][64];
  float wmax[8][4];
  float wsum[8][4];
  float opart[8][512];
  float wgt[8][32];
  float orow[8][32];
  float wpart[8][32];
  float hs[512];
  float upart[4][32];
  float gs[4][32];
  float fpart[8][64];
  unsigned tag[4];
  float pad[12 * 1024];
};

__global__ void __launch_bounds__(NTHR, 3)
fused_decode_kernel(const unsigned short* __restrict__ x, const unsigned short* __restrict__ kc,
                    const unsigned short* __restrict__ vc, const unsigned short* __restrict__ wq,
                    const unsigned short* __restrict__ wo, const unsigned short* __restrict__ w1,
                    const unsigned short* __restrict__ b1, const unsigned short* __restrict__ w2,
                    const unsigned short* __restrict__ b2, unsigned short* __restrict__ out,
                    unsigned char* __restrict__ ws) {
  __shared__ __attribute__((aligned(16))) Lds L;
  const int tid = threadIdx.x, lane = tid & 63;
  const int wave = __builtin_amdgcn_readfirstlane(tid >> 6);
  const int b = blockIdx.x, g = b & 7, i = b >> 3;
  const int rb = 2 * g + (i >> 4), cb = i & 15;
  u64* const h1 = (u64*)(ws + WS_H1);
  u64* const h2o = (u64*)(ws + WS_H2O);
  u64* const h2s = (u64*)(ws + WS_H2S);
  u64* const h3 = (u64*)(ws + WS_H3);
  u64* const h4 = (u64*)(ws + WS_H4);
  STAMP(0);

  if (wave >= 8) {
    const int a = wave - 8;
    const int kr = i >> 3, cq = i & 7;
    const u32x4 xv = *reinterpret_cast<const u32x4*>(x + lane * 8);
    u64 ep = 0;
    if (a == 0) ep = *reinterpret_cast<const u64*>(ws + WS_EPOCH);
    u32x4 wqv[4];
#pragma unroll
    for (int j = 0; j < 4; ++j)
      wqv[j] = ldw16(wq + (size_t)(kr * 128 + a * 32 + j * 8 + (lane >> 3)) * (HQ * DH) + g * 512 + cq * 64 + (lane & 7) * 8);
    asm volatile("" ::: "memory");
    float xf[8];
#pragma unroll
    for (int k = 0; k < 4; ++k) { xf[2 * k] = bflo(xv[k]); xf[2 * k + 1] = bfhi(xv[k]); }
    float s1 = 0.f;
#pragma unroll
    for (int k = 0; k < 8; ++k) s1 += xf[k];
    const float mean = wave_sum(s1) * (1.f / DM);
    float s2 = 0.f;
#pragma unroll
    for (int k = 0; k < 8; ++k) { const float d = xf[k] - mean; s2 += d * d; }
    const float var = wave_sum(s2) * (1.f / DM);
    const float mb = bf16r(mean), den = bf16r(sqrtf(bf16r(bf16r(var) + 1e-5f)));
    {
      f32x4 o0, o1;
#pragma unroll
      for (int k = 0; k < 4; ++k) { o0[k] = bf16r(bf16r(xf[k] - mb) / den); o1[k] = bf16r(bf16r(xf[4 + k] - mb) / den); }
      *reinterpret_cast<f32x4*>(&L.xs[a][lane * 8]) = o0;
      *reinterpret_cast<f32x4*>(&L.xs[a][lane * 8 + 4]) = o1;
    }
    if (a == 0 && lane == 0) { const u64 t = ep + 1; L.tag[0] = (unsigned)(t >> 8); L.tag[1] = (unsigned)t & 0xffu; }
    lds_fence();
    float acc[8];
#pragma unroll
    for (int k = 0; k < 8; ++k) acc[k] = 0.f;
#pragma unroll
    for (int j = 0; j < 4; ++j) {
      const float xr = L.xs[a][kr * 128 + a * 32 + j * 8 + (lane >> 3)];
#pragma unroll
      for (int k = 0; k < 4; ++k) { acc[2 * k] = fmaf(xr, bflo(wqv[j][k]), acc[2 * k]); acc[2 * k + 1] = fmaf(xr, bfhi(wqv[j][k]), acc[2 * k + 1]); }
    }
#pragma unroll
    for (int k = 0; k < 8; ++k) { float t = acc[k]; t += dpp<ROR8>(t); t = sw16_sum(t); acc[k] = sw32_sum(t); }
    if (lane < 8) {
      *reinterpret_cast<f32x4*>(&L.qpart[a][lane * 8]) = f32x4{acc[0], acc[1], acc[2], acc[3]};
      *reinterpret_cast<f32x4*>(&L.qpart[a][lane * 8 + 4]) = f32x4{acc[4], acc[5], acc[6], acc[7]};
    }
    STAMP(1);
    bar();
    Tag tg; tg.hi = L.tag[0]; tg.lo = L.tag[1];
    if (a == 0) {
      const float qv = L.qpart[0][lane] + L.qpart[1][lane] + L.qpart[2][lane] + L.qpart[3][lane];
      put(h1 + (g * 4 + kr) * 512 + cq * 64 + lane, qv, tg);
    }
    STAMP(2);
    {
      const u64* src = h1 + (size_t)g * 4 * 512 + a * 128 + lane * 2;
      u64 v[8];
      for (unsigned spins = 0; spins < SPIN_CAP; ++spins) {
        bool ok = true;
#pragma unroll
        for (int k = 0; k < 4; ++k) { v[2 * k] = get(src + k * 512); v[2 * k + 1] = get(src + k * 512 + 1); }
#pragma unroll
        for (int k = 0; k < 8; ++k) ok &= gok(v[k], tg);
        if (__all(ok)) break;
        __builtin_amdgcn_s_sleep(2);
      }
      const float q0 = (gval(v[0]) + gval(v[2])) + (gval(v[4]) + gval(v[6]));
      const float q1 = (gval(v[1]) + gval(v[3])) + (gval(v[5]) + gval(v[7]));
      L.qs[a][lane] = (__float_as_uint(bf16r(q0)) >> 16) | (__float_as_uint(bf16r(q1)) & 0xffff0000u);
    }
    STAMP(3);
    bar();
    bar();
    bar();
    if (b >= NMLP) { bar(); return; }
    u32x4 w1v[8], w2v[8];
#pragma unroll
    for (int j = 0; j < 8; ++j)
      w1v[j] = ldw16(w1 + (size_t)(a * 128 + j * 16 + (lane >> 2)) * FF + b * 32 + (lane & 3) * 8);
#pragma unroll
    for (int j = 0; j < 8; ++j)
      w2v[j] = ldw16(w2 + (size_t)(b * 32 + j * 4 + (lane >> 4)) * DM + a * 128 + (lane & 15) * 8);
    float b1v = 0.f;
    if (lane < 32) b1v = bflo(b1[b * 32 + lane]);
    asm volatile("" ::: "memory");
    bar();
    bar();
    STAMP(4);
    {
      const f32x4 h0 = *reinterpret_cast<const f32x4*>(&L.hs[lane * 8]);
      const f32x4 h1_ = *reinterpret_cast<const f32x4*>(&L.hs[lane * 8 + 4]);
      float t1 = (h0[0] + h0[1]) + (h0[2] + h0[3]) + (h1_[0] + h1_[1]) + (h1_[2] + h1_[3]);
      const float mean2 = wave_sum(t1) * (1.f / DM);
      float t2 = 0.f;
#pragma unroll
      for (int k = 0; k < 4; ++k) { float d = h0[k] - mean2; t2 += d * d; d = h1_[k] - mean2; t2 += d * d; }
      const float var2 = wave_sum(t2) * (1.f / DM);
      const float mb2 = bf16r(mean2), den2 = bf16r(sqrtf(bf16r(bf16r(var2) + 1e-5f)));
#pragma unroll
      for (int k = 0; k < 8; ++k) acc[k] = 0.f;
#pragma unroll
      for (int j = 0; j < 8; ++j) {
        const float hn = bf16r(bf16r(L.hs[a * 128 + j * 16 + (lane >> 2)] - mb2) / den2);
#pragma unroll
        for (int k = 0; k < 4; ++k) { acc[2 * k] = fmaf(hn, bflo(w1v[j][k]), acc[2 * k]); acc[2 * k + 1] = fmaf(hn, bfhi(w1v[j][k]), acc[2 * k + 1]); }
      }
#pragma unroll
      for (int k = 0; k < 8; ++k) { float t = acc[k]; t += dpp<ROR4>(t); t += dpp<ROR8>(t); t = sw16_sum(t); acc[k] = sw32_sum(t); }
      if (lane < 4) {
        *reinterpret_cast<f32x4*>(&L.upart[a][lane * 8]) = f32x4{acc[0], acc[1], acc[2], acc[3]};
        *reinterpret_cast<f32x4*>(&L.upart[a][lane * 8 + 4]) = f32x4{acc[4], acc[5], acc[6], acc[7]};
      }
    }
    bar();
    {
      if (lane < 32) {
        float u = (L.upart[0][lane] + L.upart[1][lane]) + (L.upart[2][lane] + L.upart[3][lane]);
        u = bf16r(bf16r(u) + b1v);
        const float z = 0.7978845608028654f * (u + 0.044715f * u * u * u);
        const float th = 1.f - 2.f / (__expf(2.f * z) + 1.f);
        L.gs[a][lane] = bf16r(0.5f * u * (1.f + th));
      }
      lds_fence();
#pragma unroll
      for (int k = 0; k < 8; ++k) acc[k] = 0.f;
#pragma unroll
      for (int j = 0; j < 8; ++j) {
        const float gv = L.gs[a][j * 4 + (lane >> 4)];
#pragma unroll
        for (int k = 0; k < 4; ++k) { acc[2 * k] = fmaf(gv, bflo(w2v[j][k]), acc[2 * k]); acc[2 * k + 1] = fmaf(gv, bfhi(w2v[j][k]), acc[2 * k + 1]); }
      }
#pragma unroll
      for (int k = 0; k < 8; ++k) acc[k] = sw32_sum(sw16_sum(acc[k]));
      if (lane < 16) {
#pragma unroll
        for (int k = 0; k < 8; ++k) put(h4 + (size_t)b * 512 + a * 128 + k * 16 + lane, acc[k], tg);
      }
    }
    STAMP(5);
    if (b < NFIN) bar();
    return;
  }

  const int w = wave;
  const size_t kvbase = ((size_t)g * SK + (SK - WIN) + i * 128 + w * 16) * DH;
  u32x4 kf[4];
#pragma unroll
  for (int c = 0; c < 4; ++c) kf[c] = ldw16(kc + kvbase + (size_t)(lane & 15) * DH + c * 32 + (lane >> 4) * 8);
  unsigned vv[16];
#pragma unroll
  for (int k = 0; k < 16; ++k) vv[k] = ldw4(vc + kvbase + (size_t)k * DH + lane * 2);
  u32x4 wov[2];
#pragma unroll
  for (int j = 0; j < 2; ++j)
    wov[j] = ldw16(wo + (size_t)(rb * 256 + w * 32 + j * 16 + (lane >> 2)) * DM + cb * 32 + (lane & 3) * 8);
  float xres = 0.f, b2v = 0.f;
  if (b < NMLP) xres = bflo(x[tid]);
  if (b < NFIN && tid < 64) b2v = bflo(b2[b * 64 + tid]);
  asm volatile("" ::: "memory");
  bar();
  Tag tg; tg.hi = L.tag[0]; tg.lo = L.tag[1];
  bar();
  STAMP(1);
  f32x4 s = {0.f, 0.f, 0.f, 0.f};
#pragma unroll
  for (int c = 0; c < 4; ++c) {
    u32x4 qa = {0u, 0u, 0u, 0u};
    if ((lane & 15) < 4) qa = *reinterpret_cast<const u32x4*>(&L.qs[lane & 15][c * 16 + (lane >> 4) * 4]);
    s = __builtin_amdgcn_mfma_f32_16x16x32_bf16(__builtin_bit_cast(s16x8, qa), __builtin_bit_cast(s16x8, kf[c]), s, 0, 0, 0);
  }
  float sc[4], mx[4];
#pragma unroll
  for (int h = 0; h < 4; ++h) { sc[h] = bf16r(bf16r(s[h]) * 0.08838834764831845f); mx[h] = row16_max(sc[h]); }
  if (lane == 0) *reinterpret_cast<f32x4*>(&L.wmax[w][0]) = f32x4{mx[0], mx[1], mx[2], mx[3]};
  STAMP(2);
  bar();
  {
    f32x4 m4 = *reinterpret_cast<const f32x4*>(&L.wmax[0][0]);
#pragma unroll
    for (int k = 1; k < 8; ++k) { const f32x4 t = *reinterpret_cast<const f32x4*>(&L.wmax[k][0]);
#pragma unroll
      for (int h = 0; h < 4; ++h) m4[h] = fmaxf(m4[h], t[h]); }
#pragma unroll
    for (int h = 0; h < 4; ++h) mx[h] = m4[h];
  }
  float p[4];
  {
    float ls[4];
#pragma unroll
    for (int h = 0; h < 4; ++h) { p[h] = __expf(sc[h] - mx[h]); ls[h] = row16_sum(p[h]); }
    if (lane == 0) *reinterpret_cast<f32x4*>(&L.wsum[w][0]) = f32x4{ls[0], ls[1], ls[2], ls[3]};
  }
  float oa[4][2];
#pragma unroll
  for (int h = 0; h < 4; ++h) { oa[h][0] = 0.f; oa[h][1] = 0.f; }
#pragma unroll
  for (int k = 0; k < 16; ++k) {
    const float v0 = bflo(vv[k]), v1 = bfhi(vv[k]);
#pragma unroll
    for (int h = 0; h < 4; ++h) {
      const float pk = __builtin_bit_cast(float, __builtin_amdgcn_readlane(__builtin_bit_cast(int, p[h]), k));
      oa[h][0] = fmaf(pk, v0, oa[h][0]); oa[h][1] = fmaf(pk, v1, oa[h][1]);
    }
  }
#pragma unroll
  for (int h = 0; h < 4; ++h) *reinterpret_cast<f32x2*>(&L.opart[w][h * 128 + lane * 2]) = f32x2{oa[h][0], oa[h][1]};
  STAMP(3);
  bar();
  {
    float o = 0.f;
#pragma unroll
    for (int k = 0; k < 8; ++k) o += L.opart[k][tid];
    put(h2o + ((size_t)g * 32 + i) * 512 + tid, o, tg);
    if (tid < 4) {
      float l = 0.f;
#pragma unroll
      for (int k = 0; k < 8; ++k) l += L.wsum[k][tid];
      float m = L.wmax[0][tid];
#pragma unroll
      for (int k = 1; k < 8; ++k) m = fmaxf(m, L.wmax[k][tid]);
      put(h2s + (((size_t)g * 4 + tid) * 2 + 0) * 32 + i, m, tg);
      put(h2s + (((size_t)g * 4 + tid) * 2 + 1) * 32 + i, l, tg);
    }
  }
  STAMP(4);
  {
    const int hl = (rb & 1) * 2 + (w >> 2), d = (w & 3) * 32 + (lane & 31), half = lane >> 5;
    const u64* so = h2o + ((size_t)g * 32 + half * 16) * 512 + hl * 128 + d;
    const u64* ss = h2s + ((size_t)g * 4 + hl) * 64 + lane;
    u64 v[16], st;
    for (unsigned spins = 0; spins < SPIN_CAP; ++spins) {
      bool ok = true;
      st = get(ss);
#pragma unroll
      for (int k = 0; k < 16; ++k) v[k] = get(so + k * 512);
      ok &= gok(st, tg);
#pragma unroll
      for (int k = 0; k < 16; ++k) ok &= gok(v[k], tg);
      if (__all(ok)) break;
      __builtin_amdgcn_s_sleep(1);
    }
    STAMP(5);
    const float sv = gval(st);
    const float mm = lane < 32 ? sv : -INFINITY;
    float M = sw16_max(row16_max(mm));
    M = __builtin_bit_cast(float, __builtin_amdgcn_readlane(__builtin_bit_cast(int, M), 0));
    const float wg = __expf(mm - M);
    if (lane < 32) L.wgt[w][lane] = wg;
    lds_fence();
    const float lw = lane >= 32 ? sv * L.wgt[w][lane - 32] : 0.f;
    float Ls = sw16_sum(row16_sum(lw));
    Ls = __builtin_bit_cast(float, __builtin_amdgcn_readlane(__builtin_bit_cast(int, Ls), 32));
    float o = 0.f;
#pragma unroll
    for (int k4 = 0; k4 < 4; ++k4) {
      const f32x4 wv = *reinterpret_cast<const f32x4*>(&L.wgt[w][half * 16 + k4 * 4]);
#pragma unroll
      for (int k = 0; k < 4; ++k) o = fmaf(wv[k], gval(v[k4 * 4 + k]), o);
    }
    o = sw32_sum(o);
    o = bf16r(o / Ls);
    if (lane < 32) L.orow[w][lane] = o;
    lds_fence();
    float acc[8];
#pragma unroll
    for (int k = 0; k < 8; ++k) acc[k] = 0.f;
#pragma unroll
    for (int j = 0; j < 2; ++j) {
      const float orv = L.orow[w][j * 16 + (lane >> 2)];
#pragma unroll
      for (int k = 0; k < 4; ++k) { acc[2 * k] = fmaf(orv, bflo(wov[j][k]), acc[2 * k]); acc[2 * k + 1] = fmaf(orv, bfhi(wov[j][k]), acc[2 * k + 1]); }
    }
#pragma unroll
    for (int k = 0; k < 8; ++k) { float t = acc[k]; t += dpp<ROR4>(t); t += dpp<ROR8>(t); t = sw16_sum(t); acc[k] = sw32_sum(t); }
    if (lane < 4) {
      *reinterpret_cast<f32x4*>(&L.wpart[w][lane * 8]) = f32x4{acc[0], acc[1], acc[2], acc[3]};
      *reinterpret_cast<f32x4*>(&L.wpart[w][lane * 8 + 4]) = f32x4{acc[4], acc[5], acc[6], acc[7]};
    }
  }
  bar();
  if (tid < 32) {
    float t = 0.f;
#pragma unroll
    for (int k = 0; k < 8; ++k) t += L.wpart[k][tid];
    put(h3 + (size_t)rb * 512 + cb * 32 + tid, t, tg);
  }
  STAMP(6);
  if (b >= NMLP) return;
  {
    u64 v[16];
    for (unsigned spins = 0; spins < SPIN_CAP; ++spins) {
      bool ok = true;
#pragma unroll
      for (int k = 0; k < 16; ++k) v[k] = get(h3 + k * 512 + tid);
#pragma unroll
      for (int k = 0; k < 16; ++k) ok &= gok(v[k], tg);
      if (__all(ok)) break;
      __builtin_amdgcn_s_sleep(1);
    }
    float t = 0.f;
#pragma unroll
    for (int k = 0; k < 16; ++k) t += gval(v[k]);
    L.hs[tid] = bf16r(xres + bf16r(t));
  }
  STAMP(7);
  bar();
  bar();
  if (b >= NFIN) return;
  {
    const int col = b * 64 + lane;
    const int slot = (col & ~127) + (col & 7) * 16 + ((col >> 3) & 15);
    u64 v[7];
    for (unsigned spins = 0; spins < SPIN_CAP; ++spins) {
      bool ok = true;
#pragma unroll
      for (int k = 0; k < 7; ++k) v[k] = get(h4 + (size_t)(w * 7 + k) * 512 + slot);
#pragma unroll
      for (int k = 0; k < 7; ++k) ok &= gok(v[k], tg);
      if (__all(ok)) break;
      __builtin_amdgcn_s_sleep(1);
    }
    float t = 0.f;
#pragma unroll
    for (int k = 0; k < 7; ++k) t += gval(v[k]);
    L.fpart[w][lane] = t;
  }
  STAMP(8);
  bar();
  if (tid < 64) {
    float t = 0.f;
#pragma unroll
    for (int k = 0; k < 8; ++k) t += L.fpart[k][tid];
    const float y = bf16r(bf16r(t) + b2v);
    const float r = bf16r(L.hs[b * 64 + tid] + y);
    out[b * 64 + tid] = (unsigned short)(__float_as_uint(r) >> 16);
    if (b == 0 && tid == 0) *reinterpret_cast<u64*>(ws + WS_EPOCH) = ((u64)tg.hi << 8) | tg.lo;
  }
  STAMP(9);
}

extern "C" void kernel_launch(void* const* d_in, const int* in_sizes, int n_in,
                              void* d_out, int out_size, void* d_ws, size_t ws_size,
                              hipStream_t stream) {
  const unsigned short* x = (const unsigned short*)d_in[0];
  const unsigned short* kc = (const unsigned short*)d_in[1];
  const unsigned short* vc = (const unsigned short*)d_in[2];
  const unsigned short* wq = (const unsigned short*)d_in[3];
  const unsigned short* wo = (const unsigned short*)d_in[4];
  const unsigned short* w1 = (const unsigned short*)d_in[6];
  const unsigned short* b1 = (const unsigned short*)d_in[7];
  const unsigned short* w2 = (const unsigned short*)d_in[8];
  const unsigned short* b2 = (const unsigned short*)d_in[9];
  fused_decode_kernel<<<NBLK, NTHR, 0, stream>>>(x, kc, vc, wq, wo, w1, b1, w2, b2,
                                                 (unsigned short*)d_out, (unsigned char*)d_ws);
}
```

```cpp
#include <hip/hip_runtime.h>
#include <hip/hip_bf16.h>
#include <stdint.h>

typedef unsigned long long u64;
typedef __attribute__((ext_vector_type(4))) unsigned u32x4;
typedef __attribute__((ext_vector_type(2))) unsigned u32x2;
typedef __attribute__((ext_vector_type(4))) float f32x4;
typedef __attribute__((ext_vector_type(2))) float f32x2;
typedef __attribute__((ext_vector_type(8))) short s16x8;

constexpr int DM = 512, HQ = 32, HKV = 8, SK = 65536, DH = 128, WIN = 4096, FF = 1792;
constexpr int NBLK = 256, NTHR = 512;
constexpr int NMLP = 56, NFIN = 8;

constexpr size_t WS_EPOCH = 0;
constexpr size_t WS_H1 = 4096;
constexpr size_t WS_H2O = WS_H1 + (size_t)8 * 4 * 512 * 8;
constexpr size_t WS_H2S = WS_H2O + (size_t)8 * 32 * 512 * 8;
constexpr size_t WS_H3 = WS_H2S + (size_t)8 * 32 * 8 * 8;
constexpr size_t WS_H4 = WS_H3 + (size_t)16 * 512 * 8;
constexpr size_t WS_END = WS_H4 + (size_t)NMLP * 512 * 8;

constexpr unsigned SPIN_CAP = 1u << 18;

#define KV_NT 1

__device__ __forceinline__ float bf16r(float x) { return __bfloat162float(__float2bfloat16(x)); }
__device__ __forceinline__ float bflo(unsigned u) { return __uint_as_float(u << 16); }
__device__ __forceinline__ float bfhi(unsigned u) { return __uint_as_float(u & 0xffff0000u); }
__device__ __forceinline__ unsigned pk2(float lo, float hi) {
  return (__float_as_uint(bf16r(lo)) >> 16) | (__float_as_uint(bf16r(hi)) & 0xffff0000u);
}
__device__ __forceinline__ unsigned plo(unsigned a, unsigned b) { return __builtin_amdgcn_perm(b, a, 0x05040100u); }
__device__ __forceinline__ unsigned phi(unsigned a, unsigned b) { return __builtin_amdgcn_perm(b, a, 0x07060302u); }

template <int CTRL> __device__ __forceinline__ float dpp(float x) {
  return __builtin_bit_cast(float, __builtin_amdgcn_mov_dpp(__builtin_bit_cast(int, x), CTRL, 0xf, 0xf, true));
}
constexpr int XOR1 = 0xB1, XOR2 = 0x4E, ROR4 = 0x124, ROR8 = 0x128;
__device__ __forceinline__ float sw16_sum(float x) {
  auto s = __builtin_amdgcn_permlane16_swap(__float_as_uint(x), __float_as_uint(x), false, false);
  return __uint_as_float(s[0]) + __uint_as_float(s[1]);
}
__device__ __forceinline__ float sw32_sum(float x) {
  auto s = __builtin_amdgcn_permlane32_swap(__float_as_uint(x), __float_as_uint(x), false, false);
  return __uint_as_float(s[0]) + __uint_as_float(s[1]);
}
__device__ __forceinline__ float sw16_max(float x) {
  auto s = __builtin_amdgcn_permlane16_swap(__float_as_uint(x), __float_as_uint(x), false, false);
  return fmaxf(__uint_as_float(s[0]), __uint_as_float(s[1]));
}
__device__ __forceinline__ float row16_sum(float x) {
  x += dpp<XOR1>(x); x += dpp<XOR2>(x); x += dpp<ROR4>(x); x += dpp<ROR8>(x); return x;
}
__device__ __forceinline__ float row16_max(float x) {
  x = fmaxf(x, dpp<XOR1>(x)); x = fmaxf(x, dpp<XOR2>(x)); x = fmaxf(x, dpp<ROR4>(x)); x = fmaxf(x, dpp<ROR8>(x)); return x;
}
__device__ __forceinline__ float wave_sum(float x) { return sw32_sum(sw16_sum(row16_sum(x))); }
__device__ __forceinline__ f32x4 mfma(u32x4 a, u32x4 b, f32x4 c) {
  return __builtin_amdgcn_mfma_f32_16x16x32_bf16(__builtin_bit_cast(s16x8, a), __builtin_bit_cast(s16x8, b), c, 0, 0, 0);
}

template <typename T> __device__ __forceinline__ T ldw(const void* p) {
  return __builtin_nontemporal_load(reinterpret_cast<const T*>(p));
}

__device__ __forceinline__ void put(u64* g, float v, unsigned tag) {
  __hip_atomic_store(g, ((u64)tag << 32) | (__float_as_uint(v) & 0xffffff00u), __ATOMIC_RELAXED, __HIP_MEMORY_SCOPE_AGENT);
}
__device__ __forceinline__ u64 get(const u64* g) {
  return __hip_atomic_load(g, __ATOMIC_RELAXED, __HIP_MEMORY_SCOPE_AGENT);
}
__device__ __forceinline__ float gval(u64 x) { return __uint_as_float((unsigned)x); }
template <int N> __device__ __forceinline__ bool gall(const u64 (&v)[N], unsigned tag) {
  bool ok = true; unsigned lo = 0;
#pragma unroll
  for (int k = 0; k < N; ++k) { ok &= (unsigned)(v[k] >> 32) == tag; lo |= (unsigned)v[k]; }
  ok &= (lo & 0xffu) == 0u;
  return __all(ok);
}

__device__ __forceinline__ void bar() {
  asm volatile("s_waitcnt lgkmcnt(0)" ::: "memory");
  __builtin_amdgcn_s_barrier();
  asm volatile("" ::: "memory");
}
__device__ __forceinline__ void lds_fence() { asm volatile("s_waitcnt lgkmcnt(0)" ::: "memory"); }
#define PIN() do { asm volatile("" ::: "memory"); __builtin_amdgcn_sched_barrier(0); } while (0)

#define STAMP(k) do { } while (0)

struct Lds {
  float qpart[4][64];
  unsigned qs[4][64];
  float wmax[4][4];
  float wsum[4][4];
  unsigned short ps[4][4][32];
  float opart[4][512];
  float wgt[8][32];
  unsigned short orow[8][32];
  float wpart[8][32];
  float hs[512];
  float upart[8][32];
  unsigned short gs[8][32];
  float fpart[8][64];
  unsigned tag[4];
  float pad[18 * 1024];
};

__global__ void __launch_bounds__(NTHR, 2)
fused_decode_kernel(const unsigned short* __restrict__ x, const unsigned short* __restrict__ kc,
                    const unsigned short* __restrict__ vc, const unsigned short* __restrict__ wq,
                    const unsigned short* __restrict__ wo, const unsigned short* __restrict__ w1,
                    const unsigned short* __restrict__ b1, const unsigned short* __restrict__ w2,
                    const unsigned short* __restrict__ b2, unsigned short* __restrict__ out,
                    unsigned char* __restrict__ ws) {
  __shared__ __attribute__((aligned(16))) Lds L;
  const int tid = threadIdx.x, lane = tid & 63, l15 = lane & 15, lq = lane >> 4;
  const int wave = __builtin_amdgcn_readfirstlane(tid >> 6);
  const int b = blockIdx.x, g = b & 7, i = b >> 3;
  const int rb = 2 * g + (i >> 4), cb = i & 15;
  u64* const h1 = (u64*)(ws + WS_H1);
  u64* const h2o = (u64*)(ws + WS_H2O);
  u64* const h2s = (u64*)(ws + WS_H2S);
  u64* const h3 = (u64*)(ws + WS_H3);
  u64* const h4 = (u64*)(ws + WS_H4);
  const f32x4 zero4 = {0.f, 0.f, 0.f, 0.f};
  const u32x4 zerou = {0u, 0u, 0u, 0u};
  unsigned wov[8];
  unsigned tag;
  STAMP(0);

  if (wave < 4) {
    const int a = wave, kr = i >> 3, cq = i & 7;
    const u32x4 xv = *reinterpret_cast<const u32x4*>(x + lane * 8);
    const u64 ep = get(reinterpret_cast<const u64*>(ws + WS_EPOCH));
    u32x2 wqv[8];
#pragma unroll
    for (int j = 0; j < 8; ++j)
      wqv[j] = ldw<u32x2>(wq + (size_t)(kr * 128 + a * 32 + lq * 8 + j) * (HQ * DH) + g * 512 + cq * 64 + l15 * 4);
    PIN();
    __builtin_amdgcn_s_barrier();
    PIN();
    float xf[8];
#pragma unroll
    for (int k = 0; k < 4; ++k) { xf[2 * k] = bflo(xv[k]); xf[2 * k + 1] = bfhi(xv[k]); }
    float s1 = 0.f;
#pragma unroll
    for (int k = 0; k < 8; ++k) s1 += xf[k];
    const float mean = wave_sum(s1) * (1.f / DM);
    float s2 = 0.f;
#pragma unroll
    for (int k = 0; k < 8; ++k) { const float d = xf[k] - mean; s2 += d * d; }
    const float var = wave_sum(s2) * (1.f / DM);
    const float mb = bf16r(mean), den = bf16r(sqrtf(bf16r(bf16r(var) + 1e-5f)));
    u32x4 xn;
#pragma unroll
    for (int k = 0; k < 4; ++k) xn[k] = pk2(bf16r(xf[2 * k] - mb) / den, bf16r(xf[2 * k + 1] - mb) / den);
    u32x4 xa;
    {
      const int src = (kr * 16 + a * 4 + lq) * 4;
#pragma unroll
      for (int k = 0; k < 4; ++k) { const unsigned t = (unsigned)__builtin_amdgcn_ds_bpermute(src, (int)xn[k]); xa[k] = l15 == 0 ? t : 0u; }
    }
    if (a == 0 && lane == 0) L.tag[0] = (unsigned)ep + 1u;
    {
      f32x4 qc[4];
      u32x4 f0, f1, f2, f3;
#pragma unroll
      for (int k = 0; k < 4; ++k) {
        f0[k] = plo(wqv[2 * k][0], wqv[2 * k + 1][0]); f1[k] = phi(wqv[2 * k][0], wqv[2 * k + 1][0]);
        f2[k] = plo(wqv[2 * k][1], wqv[2 * k + 1][1]); f3[k] = phi(wqv[2 * k][1], wqv[2 * k + 1][1]);
      }
      qc[0] = mfma(xa, f0, zero4); qc[1] = mfma(xa, f1, zero4); qc[2] = mfma(xa, f2, zero4); qc[3] = mfma(xa, f3, zero4);
      if (lane < 16) *reinterpret_cast<f32x4*>(&L.qpart[a][lane * 4]) = f32x4{qc[0][0], qc[1][0], qc[2][0], qc[3][0]};
    }
    STAMP(1);
    bar();
    tag = L.tag[0];
    if (a == 0) {
      const float qv = (L.qpart[0][lane] + L.qpart[1][lane]) + (L.qpart[2][lane] + L.qpart[3][lane]);
      put(h1 + (g * 4 + kr) * 512 + cq * 64 + lane, qv, tag);
    }
    STAMP(2);
    {
      const u64* src = h1 + (size_t)g * 4 * 512 + a * 128 + lane * 2;
      u64 v[8];
      for (unsigned spins = 0; spins < SPIN_CAP; ++spins) {
#pragma unroll
        for (int k = 0; k < 4; ++k) { v[2 * k] = get(src + k * 512); v[2 * k + 1] = get(src + k * 512 + 1); }
        if (gall(v, tag)) break;
        __builtin_amdgcn_s_sleep(2);
      }
      const float q0 = (gval(v[0]) + gval(v[2])) + (gval(v[4]) + gval(v[6]));
      const float q1 = (gval(v[1]) + gval(v[3])) + (gval(v[5]) + gval(v[7]));
      L.qs[a][lane] = pk2(q0, q1);
    }
    PIN();
#pragma unroll
    for (int j = 0; j < 8; ++j)
      wov[j] = ldw<unsigned>(wo + (size_t)(rb * 256 + wave * 32 + lq * 8 + j) * DM + cb * 32 + l15 * 2);
    PIN();
    STAMP(3);
    bar();
    bar();
    bar();
  } else {
    const int w4 = wave - 4;
    const size_t kvbase = ((size_t)g * SK + (SK - WIN) + i * 128 + w4 * 32) * DH;
    __builtin_amdgcn_s_barrier();
    PIN();
    u32x4 kf[2][4];
#pragma unroll
    for (int t = 0; t < 2; ++t)
#pragma unroll
      for (int c = 0; c < 4; ++c) kf[t][c] = ldw<u32x4>(kc + kvbase + (size_t)(t * 16 + l15) * DH + c * 32 + lq * 8);
    u32x4 vv[8];
#pragma unroll
    for (int j = 0; j < 8; ++j) vv[j] = ldw<u32x4>(vc + kvbase + (size_t)(lq * 8 + j) * DH + l15 * 8);
#pragma unroll
    for (int j = 0; j < 8; ++j)
      wov[j] = ldw<unsigned>(wo + (size_t)(rb * 256 + wave * 32 + lq * 8 + j) * DM + cb * 32 + l15 * 2);
    PIN();
    bar();
    tag = L.tag[0];
    bar();
    STAMP(1);
    f32x4 s0 = zero4, s1 = zero4;
#pragma unroll
    for (int c = 0; c < 4; ++c) {
      u32x4 qa = zerou;
      if (l15 < 4) qa = *reinterpret_cast<const u32x4*>(&L.qs[l15][c * 16 + lq * 4]);
      s0 = mfma(qa, kf[0][c], s0);
      s1 = mfma(qa, kf[1][c], s1);
    }
    float sc0[4], sc1[4], mx[4];
#pragma unroll
    for (int h = 0; h < 4; ++h) {
      sc0[h] = bf16r(bf16r(s0[h]) * 0.08838834764831845f);
      sc1[h] = bf16r(bf16r(s1[h]) * 0.08838834764831845f);
      mx[h] = row16_max(fmaxf(sc0[h], sc1[h]));
    }
    if (lane == 0) *reinterpret_cast<f32x4*>(&L.wmax[w4][0]) = f32x4{mx[0], mx[1], mx[2], mx[3]};
    STAMP(2);
    bar();
    {
      f32x4 m4 = *reinterpret_cast<const f32x4*>(&L.wmax[0][0]);
#pragma unroll
      for (int k = 1; k < 4; ++k) { const f32x4 t = *reinterpret_cast<const f32x4*>(&L.wmax[k][0]);
#pragma unroll
        for (int h = 0; h < 4; ++h) m4[h] = fmaxf(m4[h], t[h]); }
      float ls[4];
#pragma unroll
      for (int h = 0; h < 4; ++h) {
        const float p0 = __expf(sc0[h] - m4[h]), p1 = __expf(sc1[h] - m4[h]);
        ls[h] = row16_sum(p0 + p1);
        if (lane < 16) { const unsigned pp = pk2(p0, p1); L.ps[w4][h][lane] = (unsigned short)pp; L.ps[w4][h][16 + lane] = (unsigned short)(pp >> 16); }
      }
      if (lane == 0) *reinterpret_cast<f32x4*>(&L.wsum[w4][0]) = f32x4{ls[0], ls[1], ls[2], ls[3]};
    }
    lds_fence();
    u32x4 pa = zerou;
    if (l15 < 4) pa = *reinterpret_cast<const u32x4*>(&L.ps[w4][l15][lq * 8]);
    f32x4 oc[8];
#pragma unroll
    for (int c2 = 0; c2 < 4; ++c2) {
      u32x4 f0, f1;
#pragma unroll
      for (int k = 0; k < 4; ++k) { f0[k] = plo(vv[2 * k][c2], vv[2 * k + 1][c2]); f1[k] = phi(vv[2 * k][c2], vv[2 * k + 1][c2]); }
      oc[2 * c2] = mfma(pa, f0, zero4);
      oc[2 * c2 + 1] = mfma(pa, f1, zero4);
    }
    if (lane < 16) {
#pragma unroll
      for (int h = 0; h < 4; ++h) {
        *reinterpret_cast<f32x4*>(&L.opart[w4][h * 128 + lane * 8]) = f32x4{oc[0][h], oc[1][h], oc[2][h], oc[3][h]};
        *reinterpret_cast<f32x4*>(&L.opart[w4][h * 128 + lane * 8 + 4]) = f32x4{oc[4][h], oc[5][h], oc[6][h], oc[7][h]};
      }
    }
    STAMP(3);
    bar();
  }

  {
    const float o = (L.opart[0][tid] + L.opart[1][tid]) + (L.opart[2][tid] + L.opart[3][tid]);
    put(h2o + ((size_t)g * 32 + i) * 512 + tid, o, tag);
    if (tid < 4) {
      const float l = (L.wsum[0][tid] + L.wsum[1][tid]) + (L.wsum[2][tid] + L.wsum[3][tid]);
      const float m = fmaxf(fmaxf(L.wmax[0][tid], L.wmax[1][tid]), fmaxf(L.wmax[2][tid], L.wmax[3][tid]));
      put(h2s + (((size_t)g * 4 + tid) * 2 + 0) * 32 + i, m, tag);
      put(h2s + (((size_t)g * 4 + tid) * 2 + 1) * 32 + i, l, tag);
    }
  }
  STAMP(4);
  {
    const int hl = (rb & 1) * 2 + (wave >> 2), d = (wave & 3) * 32 + (lane & 31), half = lane >> 5;
    const u64* so = h2o + ((size_t)g * 32 + half * 16) * 512 + hl * 128 + d;
    const u64* ss = h2s + ((size_t)g * 4 + hl) * 64 + lane;
    u64 v[17];
    for (unsigned spins = 0; spins < SPIN_CAP; ++spins) {
      v[16] = get(ss);
#pragma unroll
      for (int k = 0; k < 16; ++k) v[k] = get(so + k * 512);
      if (gall(v, tag)) break;
      __builtin_amdgcn_s_sleep(1);
    }
    STAMP(5);
    const float sv = gval(v[16]);
    const float mm = lane < 32 ? sv : -INFINITY;
    float M = sw16_max(row16_max(mm));
    M = __builtin_bit_cast(float, __builtin_amdgcn_readlane(__builtin_bit_cast(int, M), 0));
    const float wg = __expf(mm - M);
    if (lane < 32) L.wgt[wave][lane] = wg;
    lds_fence();
    const float lw = lane >= 32 ? sv * L.wgt[wave][lane - 32] : 0.f;
    float Ls = sw16_sum(row16_sum(lw));
    Ls = __builtin_bit_cast(float, __builtin_amdgcn_readlane(__builtin_bit_cast(int, Ls), 32));
    float o = 0.f;
#pragma unroll
    for (int k4 = 0; k4 < 4; ++k4) {
      const f32x4 wv = *reinterpret_cast<const f32x4*>(&L.wgt[wave][half * 16 + k4 * 4]);
#pragma unroll
      for (int k = 0; k < 4; ++k) o = fmaf(wv[k], gval(v[k4 * 4 + k]), o);
    }
    o = sw32_sum(o);
    if (lane < 32) L.orow[wave][lane] = (unsigned short)(__float_as_uint(bf16r(o / Ls)) >> 16);
    lds_fence();
    u32x4 oa = zerou;
    if (l15 == 0) oa = *reinterpret_cast<const u32x4*>(&L.orow[wave][lq * 8]);
    u32x4 f0, f1;
#pragma unroll
    for (int k = 0; k < 4; ++k) { f0[k] = plo(wov[2 * k], wov[2 * k + 1]); f1[k] = phi(wov[2 * k], wov[2 * k + 1]); }
    const f32x4 c0 = mfma(oa, f0, zero4), c1 = mfma(oa, f1, zero4);
    if (lane < 16) *reinterpret_cast<f32x2*>(&L.wpart[wave][lane * 2]) = f32x2{c0[0], c1[0]};
  }
  bar();
  if (tid < 32) {
    float t = 0.f;
#pragma unroll
    for (int k = 0; k < 8; ++k) t += L.wpart[k][tid];
    put(h3 + (size_t)rb * 512 + cb * 32 + tid, t, tag);
  }
  STAMP(6);
  if (b >= NMLP) return;

  PIN();
  unsigned w1v[16];
  u32x2 w2v[8];
#pragma unroll
  for (int j = 0; j < 16; ++j)
    w1v[j] = ldw<unsigned>(w1 + (size_t)(wave * 64 + (j >> 3) * 32 + lq * 8 + (j & 7)) * FF + b * 32 + l15 * 2);
#pragma unroll
  for (int j = 0; j < 8; ++j)
    w2v[j] = ldw<u32x2>(w2 + (size_t)(b * 32 + lq * 8 + j) * DM + wave * 64 + l15 * 4);
  const unsigned short xr = x[tid];
  const unsigned short b1r = b1[b * 32 + (lane & 31)];
  const unsigned short b2r = b2[(b & (NFIN - 1)) * 64 + lane];
  PIN();
  {
    u64 v[16];
    for (unsigned spins = 0; spins < SPIN_CAP; ++spins) {
#pragma unroll
      for (int k = 0; k < 16; ++k) v[k] = get(h3 + k * 512 + tid);
      if (gall(v, tag)) break;
      __builtin_amdgcn_s_sleep(1);
    }
    float t = 0.f;
#pragma unroll
    for (int k = 0; k < 16; ++k) t += gval(v[k]);
    L.hs[tid] = bf16r(bflo(xr) + bf16r(t));
  }
  STAMP(7);
  bar();
  {
    const f32x4 h0 = *reinterpret_cast<const f32x4*>(&L.hs[lane * 8]);
    const f32x4 h1_ = *reinterpret_cast<const f32x4*>(&L.hs[lane * 8 + 4]);
    float t1 = (h0[0] + h0[1]) + (h0[2] + h0[3]) + (h1_[0] + h1_[1]) + (h1_[2] + h1_[3]);
    const float mean2 = wave_sum(t1) * (1.f / DM);
    float t2 = 0.f;
#pragma unroll
    for (int k = 0; k < 4; ++k) { float d = h0[k] - mean2; t2 += d * d; d = h1_[k] - mean2; t2 += d * d; }
    const float var2 = wave_sum(t2) * (1.f / DM);
    const float mb2 = bf16r(mean2), den2 = bf16r(sqrtf(bf16r(bf16r(var2) + 1e-5f)));
    u32x4 hn;
#pragma unroll
    for (int k = 0; k < 2; ++k) {
      hn[k] = pk2(bf16r(h0[2 * k] - mb2) / den2, bf16r(h0[2 * k + 1] - mb2) / den2);
      hn[2 + k] = pk2(bf16r(h1_[2 * k] - mb2) / den2, bf16r(h1_[2 * k + 1] - mb2) / den2);
    }
    f32x4 c0 = zero4, c1 = zero4;
#pragma unroll
    for (int ks = 0; ks < 2; ++ks) {
      u32x4 ha;
      const int src = (wave * 8 + ks * 4 + lq) * 4;
#pragma unroll
      for (int k = 0; k < 4; ++k) { const unsigned t = (unsigned)__builtin_amdgcn_ds_bpermute(src, (int)hn[k]); ha[k] = l15 == 0 ? t : 0u; }
      u32x4 f0, f1;
#pragma unroll
      for (int k = 0; k < 4; ++k) { f0[k] = plo(w1v[ks * 8 + 2 * k], w1v[ks * 8 + 2 * k + 1]); f1[k] = phi(w1v[ks * 8 + 2 * k], w1v[ks * 8 + 2 * k + 1]); }
      c0 = mfma(ha, f0, c0); c1 = mfma(ha, f1, c1);
    }
    if (lane < 16) *reinterpret_cast<f32x2*>(&L.upart[wave][lane * 2]) = f32x2{c0[0], c1[0]};
  }
  bar();
  {
    if (lane < 32) {
      float u = 0.f;
#pragma unroll
      for (int k = 0; k < 8; ++k) u += L.upart[k][lane];
      u = bf16r(bf16r(u) + bflo(b1r));
      const float z = 0.7978845608028654f * (u + 0.044715f * u * u * u);
      const float th = 1.f - 2.f / (__expf(2.f * z) + 1.f);
      L.gs[wave][lane] = (unsigned short)(__float_as_uint(bf16r(0.5f * u * (1.f + th))) >> 16);
    }
    lds_fence();
    u32x4 ga = zerou;
    if (l15 == 0) ga = *reinterpret_cast<const u32x4*>(&L.gs[wave][lq * 8]);
    u32x4 f0, f1, f2, f3;
#pragma unroll
    for (int k = 0; k < 4; ++k) {
      f0[k] = plo(w2v[2 * k][0], w2v[2 * k + 1][0]); f1[k] = phi(w2v[2 * k][0], w2v[2 * k + 1][0]);
      f2[k] = plo(w2v[2 * k][1], w2v[2 * k + 1][1]); f3[k] = phi(w2v[2 * k][1], w2v[2 * k + 1][1]);
    }
    const f32x4 c0 = mfma(ga, f0, zero4), c1 = mfma(ga, f1, zero4), c2 = mfma(ga, f2, zero4), c3 = mfma(ga, f3, zero4);
    if (lane < 16) {
      u64* dst = h4 + (size_t)b * 512 + wave * 64 + lane;
      put(dst, c0[0], tag); put(dst + 16, c1[0], tag); put(dst + 32, c2[0], tag); put(dst + 48, c3[0], tag);
    }
  }
  STAMP(8);
  if (b >= NFIN) return;
  {
    const int slot = b * 64 + (lane & 3) * 16 + (lane >> 2);
    u64 v[7];
    for (unsigned spins = 0; spins < SPIN_CAP; ++spins) {
#pragma unroll
      for (int k = 0; k < 7; ++k) v[k] = get(h4 + (size_t)(wave * 7 + k) * 512 + slot);
      if (gall(v, tag)) break;
      __builtin_amdgcn_s_sleep(1);
    }
    float t = 0.f;
#pragma unroll
    for (int k = 0; k < 7; ++k) t += gval(v[k]);
    L.fpart[wave][lane] = t;
  }
  STAMP(9);
  bar();
  if (tid < 64) {
    float t = 0.f;
#pragma unroll
    for (int k = 0; k < 8; ++k) t += L.fpart[k][tid];
    const float y = bf16r(bf16r(t) + bflo(b2r));
    const float r = bf16r(L.hs[b * 64 + tid] + y);
    out[b * 64 + tid] = (unsigned short)(__float_as_uint(r) >> 16);
    if (b == 0 && tid == 0) *reinterpret_cast<u64*>(ws + WS_EPOCH) = (u64)tag;
  }
  STAMP(10);
}

extern "C" void kernel_launch(void* const* d_in, const int* in_sizes, int n_in,
                              void* d_out, int out_size, void* d_ws, size_t ws_size,
                              hipStream_t stream) {
  const unsigned short* x = (const unsigned short*)d_in[0];
  const unsigned short* kc = (const unsigned short*)d_in[1];
  const unsigned short* vc = (const unsigned short*)d_in[2];
  const unsigned short* wq = (const unsigned short*)d_in[3];
  const unsigned short* wo = (const unsigned short*)d_in[4];
  const unsigned short* w1 = (const unsigned short*)d_in[6];
  const unsigned short* b1 = (const unsigned short*)d_in[7];
  const unsigned short* w2 = (const unsigned short*)d_in[8];
  const unsigned short* b2 = (const unsigned short*)d_in[9];
  fused_decode_kernel<<<NBLK, NTHR, 0, stream>>>(x, kc, vc, wq, wo, w1, b1, w2, b2,
                                                 (unsigned short*)d_out, (unsigned char*)d_ws);
}
```

```cpp
#include <hip/hip_runtime.h>
#include <hip/hip_bf16.h>
#include <stdint.h>

typedef unsigned long long u64;
typedef __attribute__((ext_vector_type(4))) unsigned u32x4;
typedef __attribute__((ext_vector_type(2))) unsigned u32x2;
typedef __attribute__((ext_vector_type(4))) float f32x4;
typedef __attribute__((ext_vector_type(2))) float f32x2;
typedef __attribute__((ext_vector_type(8))) short s16x8;

constexpr int DM = 512, HQ = 32, HKV = 8, SK = 65536, DH = 128, WIN = 4096, FF = 1792;
constexpr int NBLK = 256, NTHR = 512;
constexpr int NMLP = 56, NFIN = 8;

constexpr size_t WS_EPOCH = 0;
constexpr size_t WS_H1 = 4096;
constexpr size_t WS_H2O = WS_H1 + (size_t)8 * 4 * 512 * 8;
constexpr size_t WS_H2S = WS_H2O + (size_t)8 * 32 * 512 * 8;
constexpr size_t WS_H3 = WS_H2S + (size_t)8 * 32 * 8 * 8;
constexpr size_t WS_H4 = WS_H3 + (size_t)16 * 512 * 8;
constexpr size_t WS_END = WS_H4 + (size_t)NMLP * 512 * 8;

constexpr unsigned SPIN_CAP = 1u << 18;
#define QDELAY 100
#define VEARLY 4

#define KV_NT 1

__device__ __forceinline__ float bf16r(float x) { return __bfloat162float(__float2bfloat16(x)); }
__device__ __forceinline__ float bflo(unsigned u) { return __uint_as_float(u << 16); }
__device__ __forceinline__ float bfhi(unsigned u) { return __uint_as_float(u & 0xffff0000u); }
__device__ __forceinline__ unsigned pk2(float lo, float hi) {
  return (__float_as_uint(bf16r(lo)) >> 16) | (__float_as_uint(bf16r(hi)) & 0xffff0000u);
}
__device__ __forceinline__ unsigned plo(unsigned a, unsigned b) { return __builtin_amdgcn_perm(b, a, 0x05040100u); }
__device__ __forceinline__ unsigned phi(unsigned a, unsigned b) { return __builtin_amdgcn_perm(b, a, 0x07060302u); }

template <int CTRL> __device__ __forceinline__ float dpp(float x) {
  return __builtin_bit_cast(float, __builtin_amdgcn_mov_dpp(__builtin_bit_cast(int, x), CTRL, 0xf, 0xf, true));
}
constexpr int XOR1 = 0xB1, XOR2 = 0x4E, ROR4 = 0x124, ROR8 = 0x128;
__device__ __forceinline__ float sw16_sum(float x) {
  auto s = __builtin_amdgcn_permlane16_swap(__float_as_uint(x), __float_as_uint(x), false, false);
  return __uint_as_float(s[0]) + __uint_as_float(s[1]);
}
__device__ __forceinline__ float sw32_sum(float x) {
  auto s = __builtin_amdgcn_permlane32_swap(__float_as_uint(x), __float_as_uint(x), false, false);
  return __uint_as_float(s[0]) + __uint_as_float(s[1]);
}
__device__ __forceinline__ float sw16_max(float x) {
  auto s = __builtin_amdgcn_permlane16_swap(__float_as_uint(x), __float_as_uint(x), false, false);
  return fmaxf(__uint_as_float(s[0]), __uint_as_float(s[1]));
}
__device__ __forceinline__ float row16_sum(float x) {
  x += dpp<XOR1>(x); x += dpp<XOR2>(x); x += dpp<ROR4>(x); x += dpp<ROR8>(x); return x;
}
__device__ __forceinline__ float row16_max(float x) {
  x = fmaxf(x, dpp<XOR1>(x)); x = fmaxf(x, dpp<XOR2>(x)); x = fmaxf(x, dpp<ROR4>(x)); x = fmaxf(x, dpp<ROR8>(x)); return x;
}
__device__ __forceinline__ float wave_sum(float x) { return sw32_sum(sw16_sum(row16_sum(x))); }
__device__ __forceinline__ f32x4 mfma(u32x4 a, u32x4 b, f32x4 c) {
  return __builtin_amdgcn_mfma_f32_16x16x32_bf16(__builtin_bit_cast(s16x8, a), __builtin_bit_cast(s16x8, b), c, 0, 0, 0);
}

template <typename T> __device__ __forceinline__ T ldw(const void* p) {
  return __builtin_nontemporal_load(reinterpret_cast<const T*>(p));
}

__device__ __forceinline__ void put(u64* g, float v, unsigned tag) {
  __hip_atomic_store(g, ((u64)tag << 32) | (__float_as_uint(v) & 0xffffff00u), __ATOMIC_RELAXED, __HIP_MEMORY_SCOPE_AGENT);
}
__device__ __forceinline__ u64 get(const u64* g) {
  return __hip_atomic_load(g, __ATOMIC_RELAXED, __HIP_MEMORY_SCOPE_AGENT);
}
__device__ __forceinline__ float gval(u64 x) { return __uint_as_float((unsigned)x); }
template <int N> __device__ __forceinline__ bool gall(const u64 (&v)[N], unsigned tag) {
  bool ok = true; unsigned lo = 0;
#pragma unroll
  for (int k = 0; k < N; ++k) { ok &= (unsigned)(v[k] >> 32) == tag; lo |= (unsigned)v[k]; }
  ok &= (lo & 0xffu) == 0u;
  return __all(ok);
}

__device__ __forceinline__ void bar() {
  asm volatile("s_waitcnt lgkmcnt(0)" ::: "memory");
  __builtin_amdgcn_s_barrier();
  asm volatile("" ::: "memory");
}
__device__ __forceinline__ void lds_fence() { asm volatile("s_waitcnt lgkmcnt(0)" ::: "memory"); }
#define PIN() do { asm volatile("" ::: "memory"); __builtin_amdgcn_sched_barrier(0); } while (0)

#define STAMP(k) do { } while (0)

struct Lds {
  float qpart[4][64];
  unsigned qs[4][64];
  float wmax[4][4];
  float wsum[4][4];
  unsigned short ps[4][4][32];
  float opart[4][512];
  float wgt[8][32];
  unsigned short orow[8][32];
  float wpart[8][64];
  float hs[512];
  float hstat[8][2];
  unsigned short hn[8][64];
  float upart[8][32];
  unsigned short gs[8][32];
  float fpart[8][64];
  unsigned tag[4];
  unsigned qcnt[4];
  float pad[18 * 1024];
};

__global__ void __launch_bounds__(NTHR, 2)
fused_decode_kernel(const unsigned short* __restrict__ x, const unsigned short* __restrict__ kc,
                    const unsigned short* __restrict__ vc, const unsigned short* __restrict__ wq,
                    const unsigned short* __restrict__ wo, const unsigned short* __restrict__ w1,
                    const unsigned short* __restrict__ b1, const unsigned short* __restrict__ w2,
                    const unsigned short* __restrict__ b2, unsigned short* __restrict__ out,
                    unsigned char* __restrict__ ws) {
  __shared__ __attribute__((aligned(16))) Lds L;
  const int tid = threadIdx.x, lane = tid & 63, l15 = lane & 15, lq = lane >> 4;
  const int wave = __builtin_amdgcn_readfirstlane(tid >> 6);
  const int b = blockIdx.x, g = b & 7, i = b >> 3;
  u64* const h1 = (u64*)(ws + WS_H1);
  u64* const h2o = (u64*)(ws + WS_H2O);
  u64* const h2s = (u64*)(ws + WS_H2S);
  u64* const h3 = (u64*)(ws + WS_H3);
  u64* const h4 = (u64*)(ws + WS_H4);
  const f32x4 zero4 = {0.f, 0.f, 0.f, 0.f};
  const u32x4 zerou = {0u, 0u, 0u, 0u};
  unsigned tag;
  const int rb = 2 * g + ((i >> 3) & 1), cb = i & 7;
  const int mb_ = (i - 16) * 8 + g;
  u32x2 wov[8];
  unsigned w1v[16];
  u32x2 w2v[8];
  STAMP(0);

  if (wave < 4) {
    const int a = wave, kr = i >> 3, cq = i & 7;
    const u32x4 xv = *reinterpret_cast<const u32x4*>(x + lane * 8);
    const u64 ep = get(reinterpret_cast<const u64*>(ws + WS_EPOCH));
    u32x2 wqv[8];
#pragma unroll
    for (int j = 0; j < 8; ++j)
      wqv[j] = ldw<u32x2>(wq + (size_t)(kr * 128 + a * 32 + lq * 8 + j) * (HQ * DH) + g * 512 + cq * 64 + l15 * 4);
    if (tid == 0) L.qcnt[0] = 0u;
    PIN();
    bar();
    PIN();
    float xf[8];
#pragma unroll
    for (int k = 0; k < 4; ++k) { xf[2 * k] = bflo(xv[k]); xf[2 * k + 1] = bfhi(xv[k]); }
    float s1 = 0.f;
#pragma unroll
    for (int k = 0; k < 8; ++k) s1 += xf[k];
    const float mean = wave_sum(s1) * (1.f / DM);
    float s2 = 0.f;
#pragma unroll
    for (int k = 0; k < 8; ++k) { const float d = xf[k] - mean; s2 += d * d; }
    const float var = wave_sum(s2) * (1.f / DM);
    const float mb = bf16r(mean), rden = __builtin_amdgcn_rcpf(bf16r(__builtin_amdgcn_sqrtf(bf16r(bf16r(var) + 1e-5f))));
    u32x4 xn;
#pragma unroll
    for (int k = 0; k < 4; ++k) xn[k] = pk2(bf16r(xf[2 * k] - mb) * rden, bf16r(xf[2 * k + 1] - mb) * rden);
    u32x4 xa;
    {
      const int src = (kr * 16 + a * 4 + lq) * 4;
#pragma unroll
      for (int k = 0; k < 4; ++k) { const unsigned t = (unsigned)__builtin_amdgcn_ds_bpermute(src, (int)xn[k]); xa[k] = l15 == 0 ? t : 0u; }
    }
    tag = (unsigned)ep + 1u;
    if (tid == 0) L.tag[0] = tag;
    {
      f32x4 qc[4];
      u32x4 f0, f1, f2, f3;
#pragma unroll
      for (int k = 0; k < 4; ++k) {
        f0[k] = plo(wqv[2 * k][0], wqv[2 * k + 1][0]); f1[k] = phi(wqv[2 * k][0], wqv[2 * k + 1][0]);
        f2[k] = plo(wqv[2 * k][1], wqv[2 * k + 1][1]); f3[k] = phi(wqv[2 * k][1], wqv[2 * k + 1][1]);
      }
      qc[0] = mfma(xa, f0, zero4); qc[1] = mfma(xa, f1, zero4); qc[2] = mfma(xa, f2, zero4); qc[3] = mfma(xa, f3, zero4);
      if (lane < 16) *reinterpret_cast<f32x4*>(&L.qpart[a][lane * 4]) = f32x4{qc[0][0], qc[1][0], qc[2][0], qc[3][0]};
    }
    STAMP(1);
    lds_fence();
    if (a != 0) {
      if (lane == 0) __hip_atomic_fetch_add(&L.qcnt[0], 1u, __ATOMIC_RELAXED, __HIP_MEMORY_SCOPE_WORKGROUP);
    } else {
      for (unsigned spins = 0; spins < SPIN_CAP; ++spins) {
        if (__hip_atomic_load(&L.qcnt[0], __ATOMIC_RELAXED, __HIP_MEMORY_SCOPE_WORKGROUP) == 3u) break;
      }
      asm volatile("" ::: "memory");
      const float qv = (L.qpart[0][lane] + L.qpart[1][lane]) + (L.qpart[2][lane] + L.qpart[3][lane]);
      put(h1 + (g * 4 + kr) * 512 + cq * 64 + lane, qv, tag);
    }
    PIN();
    bar();
    PIN();
    STAMP(2);
    {
      const u64* src = h1 + (size_t)g * 4 * 512 + a * 128 + lane * 2;
      u64 v[8];
      {
        const u64 t0 = __builtin_amdgcn_s_memrealtime();
        while (__builtin_amdgcn_s_memrealtime() - t0 < QDELAY) __builtin_amdgcn_s_sleep(1);
      }
      PIN();
#pragma unroll
      for (int k = 0; k < 4; ++k) { v[2 * k] = get(src + k * 512); v[2 * k + 1] = get(src + k * 512 + 1); }
      PIN();
      bar();
      PIN();
      for (unsigned spins = 0; spins < SPIN_CAP; ++spins) {
        if (gall(v, tag)) break;
#pragma unroll
        for (int k = 0; k < 4; ++k) { v[2 * k] = get(src + k * 512); v[2 * k + 1] = get(src + k * 512 + 1); }
      }
      const float q0 = (gval(v[0]) + gval(v[2])) + (gval(v[4]) + gval(v[6]));
      const float q1 = (gval(v[1]) + gval(v[3])) + (gval(v[5]) + gval(v[7]));
      L.qs[a][lane] = pk2(q0, q1);
    }
    STAMP(3);
    bar();
    PIN();
    if (i < 16) {
#pragma unroll
      for (int j = 0; j < 8; ++j)
        wov[j] = ldw<u32x2>(wo + (size_t)(rb * 256 + wave * 32 + lq * 8 + j) * DM + cb * 64 + l15 * 4);
    } else if (mb_ < NMLP) {
#pragma unroll
      for (int j = 0; j < 16; ++j)
        w1v[j] = ldw<unsigned>(w1 + (size_t)(wave * 64 + (j >> 3) * 32 + lq * 8 + (j & 7)) * FF + mb_ * 32 + l15 * 2);
#pragma unroll
      for (int j = 0; j < 8; ++j)
        w2v[j] = ldw<u32x2>(w2 + (size_t)(mb_ * 32 + lq * 8 + j) * DM + wave * 64 + l15 * 4);
    }
    PIN();
    bar();
    bar();
  } else {
    const int w4 = wave - 4;
    const size_t kvbase = ((size_t)g * SK + (SK - WIN) + i * 128 + w4 * 32) * DH;
    bar();
    PIN();
    u32x4 kf[2][4];
    u32x4 vv[8];
#pragma unroll
    for (int c = 0; c < 4; ++c) kf[0][c] = ldw<u32x4>(kc + kvbase + (size_t)l15 * DH + c * 32 + lq * 8);
    PIN();
    bar();
    PIN();
#pragma unroll
    for (int c = 0; c < 4; ++c) kf[1][c] = ldw<u32x4>(kc + kvbase + (size_t)(16 + l15) * DH + c * 32 + lq * 8);
#pragma unroll
    for (int j = 0; j < VEARLY; ++j) vv[j] = ldw<u32x4>(vc + kvbase + (size_t)(lq * 8 + j) * DH + l15 * 8);
    PIN();
    bar();
    PIN();
#pragma unroll
    for (int j = VEARLY; j < 8; ++j) vv[j] = ldw<u32x4>(vc + kvbase + (size_t)(lq * 8 + j) * DH + l15 * 8);
#pragma unroll
    for (int j = 0; j < 8; ++j)
      wov[j] = ldw<u32x2>(wo + (size_t)(rb * 256 + wave * 32 + lq * 8 + j) * DM + cb * 64 + l15 * 4);
    PIN();
    tag = L.tag[0];
    bar();
    STAMP(1);
    f32x4 s0 = zero4, s1 = zero4;
#pragma unroll
    for (int c = 0; c < 4; ++c) {
      u32x4 qa = zerou;
      if (l15 < 4) qa = *reinterpret_cast<const u32x4*>(&L.qs[l15][c * 16 + lq * 4]);
      s0 = mfma(qa, kf[0][c], s0);
      s1 = mfma(qa, kf[1][c], s1);
    }
    float sc0[4], sc1[4], mx[4];
#pragma unroll
    for (int h = 0; h < 4; ++h) {
      sc0[h] = bf16r(bf16r(s0[h]) * 0.08838834764831845f);
      sc1[h] = bf16r(bf16r(s1[h]) * 0.08838834764831845f);
      mx[h] = row16_max(fmaxf(sc0[h], sc1[h]));
    }
    if (lane == 0) *reinterpret_cast<f32x4*>(&L.wmax[w4][0]) = f32x4{mx[0], mx[1], mx[2], mx[3]};
    STAMP(2);
    bar();
    {
      f32x4 m4 = *reinterpret_cast<const f32x4*>(&L.wmax[0][0]);
#pragma unroll
      for (int k = 1; k < 4; ++k) { const f32x4 t = *reinterpret_cast<const f32x4*>(&L.wmax[k][0]);
#pragma unroll
        for (int h = 0; h < 4; ++h) m4[h] = fmaxf(m4[h], t[h]); }
      float ls[4];
#pragma unroll
      for (int h = 0; h < 4; ++h) {
        const float p0 = __expf(sc0[h] - m4[h]), p1 = __expf(sc1[h] - m4[h]);
        ls[h] = row16_sum(p0 + p1);
        if (lane < 16) { const unsigned pp = pk2(p0, p1); L.ps[w4][h][lane] = (unsigned short)pp; L.ps[w4][h][16 + lane] = (unsigned short)(pp >> 16); }
      }
      if (lane == 0) *reinterpret_cast<f32x4*>(&L.wsum[w4][0]) = f32x4{ls[0], ls[1], ls[2], ls[3]};
    }
    lds_fence();
    u32x4 pa = zerou;
    if (l15 < 4) pa = *reinterpret_cast<const u32x4*>(&L.ps[w4][l15][lq * 8]);
    f32x4 oc[8];
#pragma unroll
    for (int c2 = 0; c2 < 4; ++c2) {
      u32x4 f0, f1;
#pragma unroll
      for (int k = 0; k < 4; ++k) { f0[k] = plo(vv[2 * k][c2], vv[2 * k + 1][c2]); f1[k] = phi(vv[2 * k][c2], vv[2 * k + 1][c2]); }
      oc[2 * c2] = mfma(pa, f0, zero4);
      oc[2 * c2 + 1] = mfma(pa, f1, zero4);
    }
    if (lane < 16) {
#pragma unroll
      for (int h = 0; h < 4; ++h) {
        *reinterpret_cast<f32x4*>(&L.opart[w4][h * 128 + lane * 8]) = f32x4{oc[0][h], oc[1][h], oc[2][h], oc[3][h]};
        *reinterpret_cast<f32x4*>(&L.opart[w4][h * 128 + lane * 8 + 4]) = f32x4{oc[4][h], oc[5][h], oc[6][h], oc[7][h]};
      }
    }
    STAMP(3);
    bar();
  }

  {
    const float o = (L.opart[0][tid] + L.opart[1][tid]) + (L.opart[2][tid] + L.opart[3][tid]);
    put(h2o + ((size_t)g * 32 + i) * 512 + tid, o, tag);
    if (tid < 4) {
      const float l = (L.wsum[0][tid] + L.wsum[1][tid]) + (L.wsum[2][tid] + L.wsum[3][tid]);
      const float m = fmaxf(fmaxf(L.wmax[0][tid], L.wmax[1][tid]), fmaxf(L.wmax[2][tid], L.wmax[3][tid]));
      put(h2s + (((size_t)g * 4 + tid) * 2 + 0) * 32 + i, m, tag);
      put(h2s + (((size_t)g * 4 + tid) * 2 + 1) * 32 + i, l, tag);
    }
  }
  STAMP(4);
  if (i < 16) {
    const int hl = (rb & 1) * 2 + (wave >> 2), d = (wave & 3) * 32 + (lane & 31), half = lane >> 5;
    const u64* so = h2o + ((size_t)g * 32 + half * 16) * 512 + hl * 128 + d;
    const u64* ss = h2s + ((size_t)g * 4 + hl) * 64 + lane;
    u64 v[17];
    v[16] = get(ss);
#pragma unroll
    for (int k = 0; k < 16; ++k) v[k] = get(so + k * 512);
    u32x4 f0, f1, f2, f3;
#pragma unroll
    for (int k = 0; k < 4; ++k) {
      f0[k] = plo(wov[2 * k][0], wov[2 * k + 1][0]); f1[k] = phi(wov[2 * k][0], wov[2 * k + 1][0]);
      f2[k] = plo(wov[2 * k][1], wov[2 * k + 1][1]); f3[k] = phi(wov[2 * k][1], wov[2 * k + 1][1]);
    }
    for (unsigned spins = 0; spins < SPIN_CAP; ++spins) {
      const bool okk = gall(v, tag);
      if (okk) break;
      __builtin_amdgcn_s_sleep(1);
      v[16] = get(ss);
#pragma unroll
      for (int k = 0; k < 16; ++k) v[k] = get(so + k * 512);
    }
    STAMP(5);
    const float sv = gval(v[16]);
    const float mm = lane < 32 ? sv : -INFINITY;
    float M = sw16_max(row16_max(mm));
    M = __builtin_bit_cast(float, __builtin_amdgcn_readlane(__builtin_bit_cast(int, M), 0));
    const float wg = __expf(mm - M);
    if (lane < 32) L.wgt[wave][lane] = wg;
    lds_fence();
    const float lw = lane >= 32 ? sv * L.wgt[wave][lane - 32] : 0.f;
    float Ls = sw16_sum(row16_sum(lw));
    Ls = __builtin_bit_cast(float, __builtin_amdgcn_readlane(__builtin_bit_cast(int, Ls), 32));
    float o = 0.f;
#pragma unroll
    for (int k4 = 0; k4 < 4; ++k4) {
      const f32x4 wv = *reinterpret_cast<const f32x4*>(&L.wgt[wave][half * 16 + k4 * 4]);
#pragma unroll
      for (int k = 0; k < 4; ++k) o = fmaf(wv[k], gval(v[k4 * 4 + k]), o);
    }
    o = sw32_sum(o);
    if (lane < 32) L.orow[wave][lane] = (unsigned short)(__float_as_uint(bf16r(o * __builtin_amdgcn_rcpf(Ls))) >> 16);
    lds_fence();
    u32x4 oa = zerou;
    if (l15 == 0) oa = *reinterpret_cast<const u32x4*>(&L.orow[wave][lq * 8]);
    const f32x4 c0 = mfma(oa, f0, zero4), c1 = mfma(oa, f1, zero4), c2 = mfma(oa, f2, zero4), c3 = mfma(oa, f3, zero4);
    if (lane < 16) *reinterpret_cast<f32x4*>(&L.wpart[wave][lane * 4]) = f32x4{c0[0], c1[0], c2[0], c3[0]};
    bar();
    if (tid < 64) {
      float t = 0.f;
#pragma unroll
      for (int k = 0; k < 8; ++k) t += L.wpart[k][tid];
      put(h3 + (size_t)rb * 512 + cb * 64 + tid, t, tag);
    }
    STAMP(6);
    return;
  }
  if (mb_ >= NMLP) return;

  PIN();
  if (wave >= 4) {
#pragma unroll
    for (int j = 0; j < 16; ++j)
      w1v[j] = ldw<unsigned>(w1 + (size_t)(wave * 64 + (j >> 3) * 32 + lq * 8 + (j & 7)) * FF + mb_ * 32 + l15 * 2);
#pragma unroll
    for (int j = 0; j < 8; ++j)
      w2v[j] = ldw<u32x2>(w2 + (size_t)(mb_ * 32 + lq * 8 + j) * DM + wave * 64 + l15 * 4);
  }
  const unsigned short xr = x[tid];
  const unsigned short b1r = b1[mb_ * 32 + (lane & 31)];
  const unsigned short b2r = b2[(mb_ & (NFIN - 1)) * 64 + lane];
  PIN();
  u32x4 e0[2], e1[2], f0, f1, f2, f3;
#pragma unroll
  for (int ks = 0; ks < 2; ++ks)
#pragma unroll
    for (int k = 0; k < 4; ++k) { e0[ks][k] = plo(w1v[ks * 8 + 2 * k], w1v[ks * 8 + 2 * k + 1]); e1[ks][k] = phi(w1v[ks * 8 + 2 * k], w1v[ks * 8 + 2 * k + 1]); }
#pragma unroll
  for (int k = 0; k < 4; ++k) {
    f0[k] = plo(w2v[2 * k][0], w2v[2 * k + 1][0]); f1[k] = phi(w2v[2 * k][0], w2v[2 * k + 1][0]);
    f2[k] = plo(w2v[2 * k][1], w2v[2 * k + 1][1]); f3[k] = phi(w2v[2 * k][1], w2v[2 * k + 1][1]);
  }
  PIN();
  float hv;
  {
    u64 v[16];
    for (unsigned spins = 0; spins < SPIN_CAP; ++spins) {
#pragma unroll
      for (int k = 0; k < 16; ++k) v[k] = get(h3 + k * 512 + tid);
      if (gall(v, tag)) break;
      __builtin_amdgcn_s_sleep(1);
    }
    float t = 0.f;
#pragma unroll
    for (int k = 0; k < 16; ++k) t += gval(v[k]);
    hv = bf16r(bflo(xr) + bf16r(t));
    L.hs[tid] = hv;
    const float a1 = wave_sum(hv), a2 = wave_sum(hv * hv);
    if (lane == 0) *reinterpret_cast<f32x2*>(&L.hstat[wave][0]) = f32x2{a1, a2};
  }
  STAMP(7);
  bar();
  {
    float t1 = 0.f, t2 = 0.f;
#pragma unroll
    for (int k = 0; k < 4; ++k) { const f32x4 t = *reinterpret_cast<const f32x4*>(&L.hstat[2 * k][0]); t1 += t[0] + t[2]; t2 += t[1] + t[3]; }
    const float mean2 = t1 * (1.f / DM);
    const float var2 = fmaxf(t2 * (1.f / DM) - mean2 * mean2, 0.f);
    const float mb2 = bf16r(mean2), rden2 = __builtin_amdgcn_rcpf(bf16r(__builtin_amdgcn_sqrtf(bf16r(bf16r(var2) + 1e-5f))));
    L.hn[wave][lane] = (unsigned short)(__float_as_uint(bf16r(bf16r(hv - mb2) * rden2)) >> 16);
    lds_fence();
    f32x4 c0 = zero4, c1 = zero4;
#pragma unroll
    for (int ks = 0; ks < 2; ++ks) {
      u32x4 ha = zerou;
      if (l15 == 0) ha = *reinterpret_cast<const u32x4*>(&L.hn[wave][ks * 32 + lq * 8]);
      c0 = mfma(ha, e0[ks], c0); c1 = mfma(ha, e1[ks], c1);
    }
    if (lane < 16) *reinterpret_cast<f32x2*>(&L.upart[wave][lane * 2]) = f32x2{c0[0], c1[0]};
  }
  bar();
  {
    if (lane < 32) {
      float u = 0.f;
#pragma unroll
      for (int k = 0; k < 8; ++k) u += L.upart[k][lane];
      u = bf16r(bf16r(u) + bflo(b1r));
      const float z = 0.7978845608028654f * (u + 0.044715f * u * u * u);
      const float th = 1.f - 2.f * __builtin_amdgcn_rcpf(__expf(2.f * z) + 1.f);
      L.gs[wave][lane] = (unsigned short)(__float_as_uint(bf16r(0.5f * u * (1.f + th))) >> 16);
    }
    lds_fence();
    u32x4 ga = zerou;
    if (l15 == 0) ga = *reinterpret_cast<const u32x4*>(&L.gs[wave][lq * 8]);
    const f32x4 c0 = mfma(ga, f0, zero4), c1 = mfma(ga, f1, zero4), c2 = mfma(ga, f2, zero4), c3 = mfma(ga, f3, zero4);
    if (lane < 16) {
      u64* dst = h4 + (size_t)mb_ * 512 + wave * 64 + lane;
      put(dst, c0[0], tag); put(dst + 16, c1[0], tag); put(dst + 32, c2[0], tag); put(dst + 48, c3[0], tag);
    }
  }
  STAMP(8);
  if (mb_ >= NFIN) return;
  {
    const int slot = mb_ * 64 + (lane & 3) * 16 + (lane >> 2);
    u64 v[7];
    for (unsigned spins = 0; spins < SPIN_CAP; ++spins) {
#pragma unroll
      for (int k = 0; k < 7; ++k) v[k] = get(h4 + (size_t)(wave * 7 + k) * 512 + slot);
      if (gall(v, tag)) break;
      __builtin_amdgcn_s_sleep(1);
    }
    float t = 0.f;
#pragma unroll
    for (int k = 0; k < 7; ++k) t += gval(v[k]);
    L.fpart[wave][lane] = t;
  }
  STAMP(9);
  bar();
  if (tid < 64) {
    float t = 0.f;
#pragma unroll
    for (int k = 0; k < 8; ++k) t += L.fpart[k][tid];
    const float y = bf16r(bf16r(t) + bflo(b2r));
    const float r = bf16r(L.hs[mb_ * 64 + tid] + y);
    out[mb_ * 64 + tid] = (unsigned short)(__float_as_uint(r) >> 16);
    if (mb_ == 0 && tid == 0) *reinterpret_cast<u64*>(ws + WS_EPOCH) = (u64)tag;
  }
  STAMP(10);
}

extern "C" void kernel_launch(void* const* d_in, const int* in_sizes, int n_in,
                              void* d_out, int out_size, void* d_ws, size_t ws_size,
                              hipStream_t stream) {
  const unsigned short* x = (const unsigned short*)d_in[0];
  const unsigned short* kc = (const unsigned short*)d_in[1];
  const unsigned short* vc = (const unsigned short*)d_in[2];
  const unsigned short* wq = (const unsigned short*)d_in[3];
  const unsigned short* wo = (const unsigned short*)d_in[4];
  const unsigned short* w1 = (const unsigned short*)d_in[6];
  const unsigned short* b1 = (const unsigned short*)d_in[7];
  const unsigned short* w2 = (const unsigned short*)d_in[8];
  const unsigned short* b2 = (const unsigned short*)d_in[9];
  fused_decode_kernel<<<NBLK, NTHR, 0, stream>>>(x, kc, vc, wq, wo, w1, b1, w2, b2,
                                                 (unsigned short*)d_out, (unsigned char*)d_ws);
}
```

```cpp
#include <hip/hip_runtime.h>
#include <hip/hip_bf16.h>
#include <stdint.h>

typedef unsigned long long u64;
typedef __attribute__((ext_vector_type(4))) unsigned u32x4;
typedef __attribute__((ext_vector_type(2))) unsigned u32x2;
typedef __attribute__((ext_vector_type(4))) float f32x4;
typedef __attribute__((ext_vector_type(2))) float f32x2;
typedef __attribute__((ext_vector_type(8))) short s16x8;

constexpr int DM = 512, HQ = 32, HKV = 8, SK = 65536, DH = 128, WIN = 4096, FF = 1792;
constexpr int NBLK = 256, NTHR = 512;
constexpr int NMLP = 56, NFIN = 8;

constexpr size_t WS_EPOCH = 0;
constexpr size_t WS_H1 = 4096;
constexpr size_t WS_H2O = WS_H1 + (size_t)8 * 4 * 512 * 8;
constexpr int H2B = 528;
constexpr size_t WS_H3 = WS_H2O + (size_t)8 * 32 * H2B * 8;
constexpr size_t WS_H4 = WS_H3 + (size_t)16 * 512 * 8;
constexpr size_t WS_END = WS_H4 + (size_t)NMLP * 512 * 8;

constexpr unsigned SPIN_CAP = 1u << 18;
#define QDELAY 100
#define H2MODE 50
#define VEARLY 0

#define KV_NT 1

__device__ __forceinline__ float bf16r(float x) { return __bfloat162float(__float2bfloat16(x)); }
__device__ __forceinline__ float bflo(unsigned u) { return __uint_as_float(u << 16); }
__device__ __forceinline__ float bfhi(unsigned u) { return __uint_as_float(u & 0xffff0000u); }
__device__ __forceinline__ unsigned pk2(float lo, float hi) {
  return (__float_as_uint(bf16r(lo)) >> 16) | (__float_as_uint(bf16r(hi)) & 0xffff0000u);
}
__device__ __forceinline__ unsigned plo(unsigned a, unsigned b) { return __builtin_amdgcn_perm(b, a, 0x05040100u); }
__device__ __forceinline__ unsigned phi(unsigned a, unsigned b) { return __builtin_amdgcn_perm(b, a, 0x07060302u); }

template <int CTRL> __device__ __forceinline__ float dpp(float x) {
  return __builtin_bit_cast(float, __builtin_amdgcn_mov_dpp(__builtin_bit_cast(int, x), CTRL, 0xf, 0xf, true));
}
constexpr int XOR1 = 0xB1, XOR2 = 0x4E, ROR4 = 0x124, ROR8 = 0x128;
__device__ __forceinline__ float sw16_sum(float x) {
  auto s = __builtin_amdgcn_permlane16_swap(__float_as_uint(x), __float_as_uint(x), false, false);
  return __uint_as_float(s[0]) + __uint_as_float(s[1]);
}
__device__ __forceinline__ float sw32_sum(float x) {
  auto s = __builtin_amdgcn_permlane32_swap(__float_as_uint(x), __float_as_uint(x), false, false);
  return __uint_as_float(s[0]) + __uint_as_float(s[1]);
}
__device__ __forceinline__ float sw16_max(float x) {
  auto s = __builtin_amdgcn_permlane16_swap(__float_as_uint(x), __float_as_uint(x), false, false);
  return fmaxf(__uint_as_float(s[0]), __uint_as_float(s[1]));
}
__device__ __forceinline__ float row16_sum(float x) {
  x += dpp<XOR1>(x); x += dpp<XOR2>(x); x += dpp<ROR4>(x); x += dpp<ROR8>(x); return x;
}
__device__ __forceinline__ float row16_max(float x) {
  x = fmaxf(x, dpp<XOR1>(x)); x = fmaxf(x, dpp<XOR2>(x)); x = fmaxf(x, dpp<ROR4>(x)); x = fmaxf(x, dpp<ROR8>(x)); return x;
}
__device__ __forceinline__ float wave_sum(float x) { return sw32_sum(sw16_sum(row16_sum(x))); }
__device__ __forceinline__ f32x4 mfma(u32x4 a, u32x4 b, f32x4 c) {
  return __builtin_amdgcn_mfma_f32_16x16x32_bf16(__builtin_bit_cast(s16x8, a), __builtin_bit_cast(s16x8, b), c, 0, 0, 0);
}

template <typename T> __device__ __forceinline__ T ldw(const void* p) {
  return __builtin_nontemporal_load(reinterpret_cast<const T*>(p));
}

__device__ __forceinline__ void put(u64* g, float v, unsigned tag) {
  __hip_atomic_store(g, ((u64)tag << 32) | (__float_as_uint(v) & 0xffffff00u), __ATOMIC_RELAXED, __HIP_MEMORY_SCOPE_AGENT);
}
__device__ __forceinline__ u64 get(const u64* g) {
  return __hip_atomic_load(g, __ATOMIC_RELAXED, __HIP_MEMORY_SCOPE_AGENT);
}
__device__ __forceinline__ float gval(u64 x) { return __uint_as_float((unsigned)x); }
__device__ __forceinline__ u32x4 get2(__amdgpu_buffer_rsrc_t r, unsigned off) {
  asm volatile("" ::: "memory");
  return __builtin_amdgcn_raw_buffer_load_b128(r, (int)off, 0, 16);
}
template <int N> __device__ __forceinline__ bool gall2(const u32x4 (&v)[N], unsigned tag) {
  bool ok = true; unsigned lo = 0;
#pragma unroll
  for (int k = 0; k < N; ++k) { ok &= (v[k][1] == tag) & (v[k][3] == tag); lo |= v[k][0] | v[k][2]; }
  ok &= (lo & 0xffu) == 0u;
  return __all(ok);
}
template <int N> __device__ __forceinline__ bool gall(const u64 (&v)[N], unsigned tag) {
  bool ok = true; unsigned lo = 0;
#pragma unroll
  for (int k = 0; k < N; ++k) { ok &= (unsigned)(v[k] >> 32) == tag; lo |= (unsigned)v[k]; }
  ok &= (lo & 0xffu) == 0u;
  return __all(ok);
}

__device__ __forceinline__ void bar() {
  asm volatile("s_waitcnt lgkmcnt(0)" ::: "memory");
  __builtin_amdgcn_s_barrier();
  asm volatile("" ::: "memory");
}
__device__ __forceinline__ void lds_fence() { asm volatile("s_waitcnt lgkmcnt(0)" ::: "memory"); }
#define PIN() do { asm volatile("" ::: "memory"); __builtin_amdgcn_sched_barrier(0); } while (0)

#define STAMP(k) do { } while (0)

struct Lds {
  float qpart[4][64];
  unsigned qs[4][64];
  float wmax[4][4];
  float wsum[4][4];
  unsigned short ps[4][4][32];
  float opart[4][512];
  float wgt[8][32];
  unsigned short orow[8][32];
  float wpart[8][64];
  float hs[512];
  float hstat[8][2];
  unsigned short hn[8][64];
  float upart[8][32];
  unsigned short gs[8][32];
  float fpart[8][64];
  unsigned tag[4];
  unsigned qcnt[4];
  float pad[18 * 1024];
};

__global__ void __launch_bounds__(NTHR, 2)
fused_decode_kernel(const unsigned short* __restrict__ x, const unsigned short* __restrict__ kc,
                    const unsigned short* __restrict__ vc, const unsigned short* __restrict__ wq,
                    const unsigned short* __restrict__ wo, const unsigned short* __restrict__ w1,
                    const unsigned short* __restrict__ b1, const unsigned short* __restrict__ w2,
                    const unsigned short* __restrict__ b2, unsigned short* __restrict__ out,
                    unsigned char* ws) {
  __shared__ __attribute__((aligned(16))) Lds L;
  const int tid = threadIdx.x, lane = tid & 63, l15 = lane & 15, lq = lane >> 4;
  const int wave = __builtin_amdgcn_readfirstlane(tid >> 6);
  const int b = blockIdx.x, g = b & 7, i = b >> 3;
  u64* const h1 = (u64*)(ws + WS_H1);
  u64* const h2o = (u64*)(ws + WS_H2O);
  u64* const h3 = (u64*)(ws + WS_H3);
  u64* const h4 = (u64*)(ws + WS_H4);
  const __amdgpu_buffer_rsrc_t wsr = __builtin_amdgcn_make_buffer_rsrc(ws, 0, (int)WS_END, 0x00020000);
  const f32x4 zero4 = {0.f, 0.f, 0.f, 0.f};
  const u32x4 zerou = {0u, 0u, 0u, 0u};
  unsigned tag;
  const int rb = 2 * g + ((i >> 3) & 1), cb = i & 7;
  const int mb_ = (i - 16) * 8 + g;
  unsigned w1v[16];
  u32x2 w2v[8];
  STAMP(0);

  if (wave < 4) {
    const int a = wave, kr = i >> 3, cq = i & 7;
    const u32x4 xv = *reinterpret_cast<const u32x4*>(x + lane * 8);
    const u64 ep = get(reinterpret_cast<const u64*>(ws + WS_EPOCH));
    u32x2 wqv[8];
#pragma unroll
    for (int j = 0; j < 8; ++j)
      wqv[j] = ldw<u32x2>(wq + (size_t)(kr * 128 + a * 32 + lq * 8 + j) * (HQ * DH) + g * 512 + cq * 64 + l15 * 4);
    if (tid == 0) L.qcnt[0] = 0u;
    PIN();
    bar();
    PIN();
    float xf[8];
#pragma unroll
    for (int k = 0; k < 4; ++k) { xf[2 * k] = bflo(xv[k]); xf[2 * k + 1] = bfhi(xv[k]); }
    float s1 = 0.f;
#pragma unroll
    for (int k = 0; k < 8; ++k) s1 += xf[k];
    const float mean = wave_sum(s1) * (1.f / DM);
    float s2 = 0.f;
#pragma unroll
    for (int k = 0; k < 8; ++k) { const float d = xf[k] - mean; s2 += d * d; }
    const float var = wave_sum(s2) * (1.f / DM);
    const float mb = bf16r(mean), rden = __builtin_amdgcn_rcpf(bf16r(__builtin_amdgcn_sqrtf(bf16r(bf16r(var) + 1e-5f))));
    u32x4 xn;
#pragma unroll
    for (int k = 0; k < 4; ++k) xn[k] = pk2(bf16r(xf[2 * k] - mb) * rden, bf16r(xf[2 * k + 1] - mb) * rden);
    u32x4 xa;
    {
      const int src = (kr * 16 + a * 4 + lq) * 4;
#pragma unroll
      for (int k = 0; k < 4; ++k) { const unsigned t = (unsigned)__builtin_amdgcn_ds_bpermute(src, (int)xn[k]); xa[k] = l15 == 0 ? t : 0u; }
    }
    tag = (unsigned)ep + 1u;
    if (tid == 0) L.tag[0] = tag;
    {
      f32x4 qc[4];
      u32x4 f0, f1, f2, f3;
#pragma unroll
      for (int k = 0; k < 4; ++k) {
        f0[k] = plo(wqv[2 * k][0], wqv[2 * k + 1][0]); f1[k] = phi(wqv[2 * k][0], wqv[2 * k + 1][0]);
        f2[k] = plo(wqv[2 * k][1], wqv[2 * k + 1][1]); f3[k] = phi(wqv[2 * k][1], wqv[2 * k + 1][1]);
      }
      qc[0] = mfma(xa, f0, zero4); qc[1] = mfma(xa, f1, zero4); qc[2] = mfma(xa, f2, zero4); qc[3] = mfma(xa, f3, zero4);
      if (lane < 16) *reinterpret_cast<f32x4*>(&L.qpart[a][lane * 4]) = f32x4{qc[0][0], qc[1][0], qc[2][0], qc[3][0]};
    }
    STAMP(1);
    lds_fence();
    if (a != 0) {
      if (lane == 0) __hip_atomic_fetch_add(&L.qcnt[0], 1u, __ATOMIC_RELAXED, __HIP_MEMORY_SCOPE_WORKGROUP);
    } else {
      for (unsigned spins = 0; spins < SPIN_CAP; ++spins) {
        if (__hip_atomic_load(&L.qcnt[0], __ATOMIC_RELAXED, __HIP_MEMORY_SCOPE_WORKGROUP) == 3u) break;
      }
      asm volatile("" ::: "memory");
      const float qv = (L.qpart[0][lane] + L.qpart[1][lane]) + (L.qpart[2][lane] + L.qpart[3][lane]);
      put(h1 + (g * 4 + kr) * 512 + cq * 64 + lane, qv, tag);
    }
    PIN();
    bar();
    PIN();
    STAMP(2);
    {
      const unsigned src = (unsigned)WS_H1 + (g * 4 * 512 + a * 128 + lane * 2) * 8;
      u32x4 v[4];
      {
        const u64 t0 = __builtin_amdgcn_s_memrealtime();
        while (__builtin_amdgcn_s_memrealtime() - t0 < QDELAY) __builtin_amdgcn_s_sleep(1);
      }
      PIN();
#pragma unroll
      for (int k = 0; k < 4; ++k) v[k] = get2(wsr, src + k * 4096);
      PIN();
      bar();
      PIN();
      for (unsigned spins = 0; spins < SPIN_CAP; ++spins) {
        if (gall2(v, tag)) break;
#pragma unroll
        for (int k = 0; k < 4; ++k) v[k] = get2(wsr, src + k * 4096);
      }
      const float q0 = (__uint_as_float(v[0][0]) + __uint_as_float(v[1][0])) + (__uint_as_float(v[2][0]) + __uint_as_float(v[3][0]));
      const float q1 = (__uint_as_float(v[0][2]) + __uint_as_float(v[1][2])) + (__uint_as_float(v[2][2]) + __uint_as_float(v[3][2]));
      L.qs[a][lane] = pk2(q0, q1);
    }
    STAMP(3);
    bar();
    PIN();
    PIN();
    bar();
    bar();
  } else {
    const int w4 = wave - 4;
    const size_t kvbase = ((size_t)g * SK + (SK - WIN) + i * 128 + w4 * 32) * DH;
    bar();
    PIN();
    u32x4 kf[2][4];
    u32x4 vv[8];
#pragma unroll
    for (int c = 0; c < 4; ++c) kf[0][c] = ldw<u32x4>(kc + kvbase + (size_t)l15 * DH + c * 32 + lq * 8);
    PIN();
    bar();
    PIN();
#pragma unroll
    for (int c = 0; c < 4; ++c) kf[1][c] = ldw<u32x4>(kc + kvbase + (size_t)(16 + l15) * DH + c * 32 + lq * 8);
#pragma unroll
    for (int j = 0; j < VEARLY; ++j) vv[j] = ldw<u32x4>(vc + kvbase + (size_t)(lq * 8 + j) * DH + l15 * 8);
    PIN();
    bar();
    PIN();
#pragma unroll
    for (int j = VEARLY; j < 8; ++j) vv[j] = ldw<u32x4>(vc + kvbase + (size_t)(lq * 8 + j) * DH + l15 * 8);
    PIN();
    tag = L.tag[0];
    bar();
    STAMP(1);
    f32x4 s0 = zero4, s1 = zero4;
#pragma unroll
    for (int c = 0; c < 4; ++c) {
      u32x4 qa = zerou;
      if (l15 < 4) qa = *reinterpret_cast<const u32x4*>(&L.qs[l15][c * 16 + lq * 4]);
      s0 = mfma(qa, kf[0][c], s0);
      s1 = mfma(qa, kf[1][c], s1);
    }
    float sc0[4], sc1[4], mx[4];
#pragma unroll
    for (int h = 0; h < 4; ++h) {
      sc0[h] = bf16r(bf16r(s0[h]) * 0.08838834764831845f);
      sc1[h] = bf16r(bf16r(s1[h]) * 0.08838834764831845f);
      mx[h] = row16_max(fmaxf(sc0[h], sc1[h]));
    }
    if (lane == 0) *reinterpret_cast<f32x4*>(&L.wmax[w4][0]) = f32x4{mx[0], mx[1], mx[2], mx[3]};
    STAMP(2);
    bar();
    {
      f32x4 m4 = *reinterpret_cast<const f32x4*>(&L.wmax[0][0]);
#pragma unroll
      for (int k = 1; k < 4; ++k) { const f32x4 t = *reinterpret_cast<const f32x4*>(&L.wmax[k][0]);
#pragma unroll
        for (int h = 0; h < 4; ++h) m4[h] = fmaxf(m4[h], t[h]); }
      float ls[4];
#pragma unroll
      for (int h = 0; h < 4; ++h) {
        const float p0 = __expf(sc0[h] - m4[h]), p1 = __expf(sc1[h] - m4[h]);
        ls[h] = row16_sum(p0 + p1);
        if (lane < 16) { const unsigned pp = pk2(p0, p1); L.ps[w4][h][lane] = (unsigned short)pp; L.ps[w4][h][16 + lane] = (unsigned short)(pp >> 16); }
      }
      if (lane == 0) *reinterpret_cast<f32x4*>(&L.wsum[w4][0]) = f32x4{ls[0], ls[1], ls[2], ls[3]};
    }
    lds_fence();
    u32x4 pa = zerou;
    if (l15 < 4) pa = *reinterpret_cast<const u32x4*>(&L.ps[w4][l15][lq * 8]);
    f32x4 oc[8];
#pragma unroll
    for (int c2 = 0; c2 < 4; ++c2) {
      u32x4 f0, f1;
#pragma unroll
      for (int k = 0; k < 4; ++k) { f0[k] = plo(vv[2 * k][c2], vv[2 * k + 1][c2]); f1[k] = phi(vv[2 * k][c2], vv[2 * k + 1][c2]); }
      oc[2 * c2] = mfma(pa, f0, zero4);
      oc[2 * c2 + 1] = mfma(pa, f1, zero4);
    }
    if (lane < 16) {
#pragma unroll
      for (int h = 0; h < 4; ++h) {
        *reinterpret_cast<f32x4*>(&L.opart[w4][h * 128 + lane * 8]) = f32x4{oc[0][h], oc[1][h], oc[2][h], oc[3][h]};
        *reinterpret_cast<f32x4*>(&L.opart[w4][h * 128 + lane * 8 + 4]) = f32x4{oc[4][h], oc[5][h], oc[6][h], oc[7][h]};
      }
    }
    STAMP(3);
    bar();
  }

  {
    const float o = (L.opart[0][tid] + L.opart[1][tid]) + (L.opart[2][tid] + L.opart[3][tid]);
    put(h2o + ((size_t)g * 32 + i) * H2B + tid, o, tag);
    if (tid < 4) {
      const float l = (L.wsum[0][tid] + L.wsum[1][tid]) + (L.wsum[2][tid] + L.wsum[3][tid]);
      const float m = fmaxf(fmaxf(L.wmax[0][tid], L.wmax[1][tid]), fmaxf(L.wmax[2][tid], L.wmax[3][tid]));
      put(h2o + ((size_t)g * 32 + i) * H2B + 512 + tid * 2, m, tag);
      put(h2o + ((size_t)g * 32 + i) * H2B + 513 + tid * 2, l, tag);
    }
  }
  STAMP(4);
  if (i < 16) {
    PIN();
    u32x2 wov[8];
#pragma unroll
    for (int j = 0; j < 8; ++j)
      wov[j] = ldw<u32x2>(wo + (size_t)(rb * 256 + wave * 32 + lq * 8 + j) * DM + cb * 64 + l15 * 4);
    PIN();
    const int hl = (rb & 1) * 2 + (wave >> 2);
    const unsigned so = (unsigned)WS_H2O + ((g * 32 + lq * 8) * H2B + hl * 128 + (wave & 3) * 32 + l15 * 2) * 8;
    const unsigned ss = (unsigned)WS_H2O + ((g * 32 + (lane & 31)) * H2B + 512 + hl * 2) * 8;
    u32x4 v[9];
    {
      const u64 t0 = __builtin_amdgcn_s_memrealtime();
      while (__builtin_amdgcn_s_memrealtime() - t0 < H2MODE) __builtin_amdgcn_s_sleep(1);
    }
    PIN();
    v[8] = get2(wsr, ss);
#pragma unroll
    for (int k = 0; k < 8; ++k) v[k] = get2(wsr, so + k * (H2B * 8));
    u32x4 f0, f1, f2, f3;
#pragma unroll
    for (int k = 0; k < 4; ++k) {
      f0[k] = plo(wov[2 * k][0], wov[2 * k + 1][0]); f1[k] = phi(wov[2 * k][0], wov[2 * k + 1][0]);
      f2[k] = plo(wov[2 * k][1], wov[2 * k + 1][1]); f3[k] = phi(wov[2 * k][1], wov[2 * k + 1][1]);
    }
    for (unsigned spins = 0; spins < SPIN_CAP; ++spins) {
      const bool okk = gall2(v, tag);
      if (okk) break;
      __builtin_amdgcn_s_sleep(1);
      v[8] = get2(wsr, ss);
#pragma unroll
      for (int k = 0; k < 8; ++k) v[k] = get2(wsr, so + k * (H2B * 8));
    }
    STAMP(5);
    const float mm = __uint_as_float(v[8][0]);
    float M = sw16_max(row16_max(mm));
    const float wg = __expf(mm - M);
    if (lane < 32) L.wgt[wave][lane] = wg;
    const float Ls = sw16_sum(row16_sum(wg * __uint_as_float(v[8][2])));
    lds_fence();
    float o0 = 0.f, o1 = 0.f;
#pragma unroll
    for (int k4 = 0; k4 < 2; ++k4) {
      const f32x4 wv = *reinterpret_cast<const f32x4*>(&L.wgt[wave][lq * 8 + k4 * 4]);
#pragma unroll
      for (int k = 0; k < 4; ++k) { o0 = fmaf(wv[k], __uint_as_float(v[k4 * 4 + k][0]), o0); o1 = fmaf(wv[k], __uint_as_float(v[k4 * 4 + k][2]), o1); }
    }
    o0 = sw32_sum(sw16_sum(o0)); o1 = sw32_sum(sw16_sum(o1));
    const float rl = __builtin_amdgcn_rcpf(Ls);
    if (lane < 16) reinterpret_cast<unsigned*>(&L.orow[wave][0])[lane] = pk2(o0 * rl, o1 * rl);
    lds_fence();
    u32x4 oa = zerou;
    if (l15 == 0) oa = *reinterpret_cast<const u32x4*>(&L.orow[wave][lq * 8]);
    const f32x4 c0 = mfma(oa, f0, zero4), c1 = mfma(oa, f1, zero4), c2 = mfma(oa, f2, zero4), c3 = mfma(oa, f3, zero4);
    if (lane < 16) *reinterpret_cast<f32x4*>(&L.wpart[wave][lane * 4]) = f32x4{c0[0], c1[0], c2[0], c3[0]};
    bar();
    if (tid < 64) {
      float t = 0.f;
#pragma unroll
      for (int k = 0; k < 8; ++k) t += L.wpart[k][tid];
      put(h3 + (size_t)rb * 512 + cb * 64 + tid, t, tag);
    }
    STAMP(6);
    return;
  }
  if (mb_ >= NMLP) return;

  PIN();
  {
#pragma unroll
    for (int j = 0; j < 16; ++j)
      w1v[j] = ldw<unsigned>(w1 + (size_t)(wave * 64 + (j >> 3) * 32 + lq * 8 + (j & 7)) * FF + mb_ * 32 + l15 * 2);
#pragma unroll
    for (int j = 0; j < 8; ++j)
      w2v[j] = ldw<u32x2>(w2 + (size_t)(mb_ * 32 + lq * 8 + j) * DM + wave * 64 + l15 * 4);
  }
  const unsigned xr = *reinterpret_cast<const unsigned*>(x + wave * 64 + (lane & 31) * 2);
  const unsigned short b1r = b1[mb_ * 32 + (lane & 31)];
  const unsigned short b2r = b2[(mb_ & (NFIN - 1)) * 64 + lane];
  PIN();
  u32x4 e0[2], e1[2], f0, f1, f2, f3;
#pragma unroll
  for (int ks = 0; ks < 2; ++ks)
#pragma unroll
    for (int k = 0; k < 4; ++k) { e0[ks][k] = plo(w1v[ks * 8 + 2 * k], w1v[ks * 8 + 2 * k + 1]); e1[ks][k] = phi(w1v[ks * 8 + 2 * k], w1v[ks * 8 + 2 * k + 1]); }
#pragma unroll
  for (int k = 0; k < 4; ++k) {
    f0[k] = plo(w2v[2 * k][0], w2v[2 * k + 1][0]); f1[k] = phi(w2v[2 * k][0], w2v[2 * k + 1][0]);
    f2[k] = plo(w2v[2 * k][1], w2v[2 * k + 1][1]); f3[k] = phi(w2v[2 * k][1], w2v[2 * k + 1][1]);
  }
  PIN();
  float hv0, hv1;
  {
    const unsigned src = (unsigned)WS_H3 + ((lane >> 5) * 8 * 512 + wave * 64 + (lane & 31) * 2) * 8;
    u32x4 v[8];
    for (unsigned spins = 0; spins < SPIN_CAP; ++spins) {
#pragma unroll
      for (int k = 0; k < 8; ++k) v[k] = get2(wsr, src + k * 4096);
      if (gall2(v, tag)) break;
      __builtin_amdgcn_s_sleep(1);
    }
    float t0 = 0.f, t1 = 0.f;
#pragma unroll
    for (int k = 0; k < 8; ++k) { t0 += __uint_as_float(v[k][0]); t1 += __uint_as_float(v[k][2]); }
    t0 = sw32_sum(t0); t1 = sw32_sum(t1);
    hv0 = bf16r(bflo(xr) + bf16r(t0));
    hv1 = bf16r(bfhi(xr) + bf16r(t1));
    if (lane < 32) *reinterpret_cast<f32x2*>(&L.hs[wave * 64 + lane * 2]) = f32x2{hv0, hv1};
    const float a1 = sw16_sum(row16_sum(hv0 + hv1)), a2 = sw16_sum(row16_sum(hv0 * hv0 + hv1 * hv1));
    if (lane == 0) *reinterpret_cast<f32x2*>(&L.hstat[wave][0]) = f32x2{a1, a2};
  }
  STAMP(7);
  bar();
  {
    float t1 = 0.f, t2 = 0.f;
#pragma unroll
    for (int k = 0; k < 4; ++k) { const f32x4 t = *reinterpret_cast<const f32x4*>(&L.hstat[2 * k][0]); t1 += t[0] + t[2]; t2 += t[1] + t[3]; }
    const float mean2 = t1 * (1.f / DM);
    const float var2 = fmaxf(t2 * (1.f / DM) - mean2 * mean2, 0.f);
    const float mb2 = bf16r(mean2), rden2 = __builtin_amdgcn_rcpf(bf16r(__builtin_amdgcn_sqrtf(bf16r(bf16r(var2) + 1e-5f))));
    if (lane < 32) reinterpret_cast<unsigned*>(&L.hn[wave][0])[lane] = pk2(bf16r(hv0 - mb2) * rden2, bf16r(hv1 - mb2) * rden2);
    lds_fence();
    f32x4 c0 = zero4, c1 = zero4;
#pragma unroll
    for (int ks = 0; ks < 2; ++ks) {
      u32x4 ha = zerou;
      if (l15 == 0) ha = *reinterpret_cast<const u32x4*>(&L.hn[wave][ks * 32 + lq * 8]);
      c0 = mfma(ha, e0[ks], c0); c1 = mfma(ha, e1[ks], c1);
    }
    if (lane < 16) *reinterpret_cast<f32x2*>(&L.upart[wave][lane * 2]) = f32x2{c0[0], c1[0]};
  }
  bar();
  {
    if (lane < 32) {
      float u = 0.f;
#pragma unroll
      for (int k = 0; k < 8; ++k) u += L.upart[k][lane];
      u = bf16r(bf16r(u) + bflo(b1r));
      const float z = 0.7978845608028654f * (u + 0.044715f * u * u * u);
      const float th = 1.f - 2.f * __builtin_amdgcn_rcpf(__expf(2.f * z) + 1.f);
      L.gs[wave][lane] = (unsigned short)(__float_as_uint(bf16r(0.5f * u * (1.f + th))) >> 16);
    }
    lds_fence();
    u32x4 ga = zerou;
    if (l15 == 0) ga = *reinterpret_cast<const u32x4*>(&L.gs[wave][lq * 8]);
    const f32x4 c0 = mfma(ga, f0, zero4), c1 = mfma(ga, f1, zero4), c2 = mfma(ga, f2, zero4), c3 = mfma(ga, f3, zero4);
    if (lane < 16) {
      u64* dst = h4 + (size_t)mb_ * 512 + wave * 64 + lane;
      put(dst, c0[0], tag); put(dst + 16, c1[0], tag); put(dst + 32, c2[0], tag); put(dst + 48, c3[0], tag);
    }
  }
  STAMP(8);
  if (mb_ >= NFIN) return;
  asm volatile("s_waitcnt vmcnt(0)" ::: "memory");
  {
    const int slot = mb_ * 64 + (lane & 3) * 16 + (lane >> 2);
    u64 v[7];
    for (unsigned spins = 0; spins < SPIN_CAP; ++spins) {
#pragma unroll
      for (int k = 0; k < 7; ++k) v[k] = get(h4 + (size_t)(wave * 7 + k) * 512 + slot);
      if (gall(v, tag)) break;
      __builtin_amdgcn_s_sleep(1);
    }
    float t = 0.f;
#pragma unroll
    for (int k = 0; k < 7; ++k) t += gval(v[k]);
    L.fpart[wave][lane] = t;
  }
  STAMP(9);
  bar();
  if (tid < 64) {
    float t = 0.f;
#pragma unroll
    for (int k = 0; k < 8; ++k) t += L.fpart[k][tid];
    const float y = bf16r(bf16r(t) + bflo(b2r));
    const float r = bf16r(L.hs[mb_ * 64 + tid] + y);
    out[mb_ * 64 + tid] = (unsigned short)(__float_as_uint(r) >> 16);
    if (mb_ == 0 && tid == 0) *reinterpret_cast<u64*>(ws + WS_EPOCH) = (u64)tag;
  }
  STAMP(10);
}

extern "C" void kernel_launch(void* const* d_in, const int* in_sizes, int n_in,
                              void* d_out, int out_size, void* d_ws, size_t ws_size,
                              hipStream_t stream) {
  const unsigned short* x = (const unsigned short*)d_in[0];
  const unsigned short* kc = (const unsigned short*)d_in[1];
  const unsigned short* vc = (const unsigned short*)d_in[2];
  const unsigned short* wq = (const unsigned short*)d_in[3];
  const unsigned short* wo = (const unsigned short*)d_in[4];
  const unsigned short* w1 = (const unsigned short*)d_in[6];
  const unsigned short* b1 = (const unsigned short*)d_in[7];
  const unsigned short* w2 = (const unsigned short*)d_in[8];
  const unsigned short* b2 = (const unsigned short*)d_in[9];
  fused_decode_kernel<<<NBLK, NTHR, 0, stream>>>(x, kc, vc, wq, wo, w1, b1, w2, b2,
                                                 (unsigned short*)d_out, (unsigned char*)d_ws);
}
```

```cpp
#include <hip/hip_runtime.h>
#include <hip/hip_bf16.h>
#include <stdint.h>

typedef unsigned long long u64;
typedef __attribute__((ext_vector_type(4))) unsigned u32x4;
typedef __attribute__((ext_vector_type(2))) unsigned u32x2;
typedef __attribute__((ext_vector_type(4))) float f32x4;
typedef __attribute__((ext_vector_type(2))) float f32x2;
typedef __attribute__((ext_vector_type(8))) short s16x8;

constexpr int DM = 512, HQ = 32, HKV = 8, SK = 65536, DH = 128, WIN = 4096, FF = 1792;
constexpr int NBLK = 256, NTHR = 512;
constexpr int NMLP = 56, NFIN = 8;

constexpr size_t WS_EPOCH = 0;
constexpr size_t WS_H1 = 4096;
constexpr size_t WS_H2O = WS_H1 + (size_t)8 * 4 * 512 * 8;
constexpr int H2B = 528;
constexpr size_t WS_H3 = WS_H2O + (size_t)8 * 32 * H2B * 8;
constexpr size_t WS_H4 = WS_H3 + (size_t)16 * 512 * 8;
constexpr size_t WS_END = WS_H4 + (size_t)NMLP * 512 * 8;

constexpr unsigned SPIN_CAP = 1u << 18;
#define QDELAY 100
#define H2MODE 50
#define PRETOUCH 0
#define CODEPF 1
constexpr unsigned CODE_BYTES = 11264;
#define VEARLY 4

#define KV_NT 1

__device__ __forceinline__ float bf16r(float x) { return __bfloat162float(__float2bfloat16(x)); }
__device__ __forceinline__ float bflo(unsigned u) { return __uint_as_float(u << 16); }
__device__ __forceinline__ float bfhi(unsigned u) { return __uint_as_float(u & 0xffff0000u); }
__device__ __forceinline__ unsigned pk2(float lo, float hi) {
  return (__float_as_uint(bf16r(lo)) >> 16) | (__float_as_uint(bf16r(hi)) & 0xffff0000u);
}
__device__ __forceinline__ unsigned plo(unsigned a, unsigned b) { return __builtin_amdgcn_perm(b, a, 0x05040100u); }
__device__ __forceinline__ unsigned phi(unsigned a, unsigned b) { return __builtin_amdgcn_perm(b, a, 0x07060302u); }

template <int CTRL> __device__ __forceinline__ float dpp(float x) {
  return __builtin_bit_cast(float, __builtin_amdgcn_mov_dpp(__builtin_bit_cast(int, x), CTRL, 0xf, 0xf, true));
}
constexpr int XOR1 = 0xB1, XOR2 = 0x4E, ROR4 = 0x124, ROR8 = 0x128;
__device__ __forceinline__ float sw16_sum(float x) {
  auto s = __builtin_amdgcn_permlane16_swap(__float_as_uint(x), __float_as_uint(x), false, false);
  return __uint_as_float(s[0]) + __uint_as_float(s[1]);
}
__device__ __forceinline__ float sw32_sum(float x) {
  auto s = __builtin_amdgcn_permlane32_swap(__float_as_uint(x), __float_as_uint(x), false, false);
  return __uint_as_float(s[0]) + __uint_as_float(s[1]);
}
__device__ __forceinline__ float sw16_max(float x) {
  auto s = __builtin_amdgcn_permlane16_swap(__float_as_uint(x), __float_as_uint(x), false, false);
  return fmaxf(__uint_as_float(s[0]), __uint_as_float(s[1]));
}
__device__ __forceinline__ float row16_sum(float x) {
  x += dpp<XOR1>(x); x += dpp<XOR2>(x); x += dpp<ROR4>(x); x += dpp<ROR8>(x); return x;
}
__device__ __forceinline__ float row16_max(float x) {
  x = fmaxf(x, dpp<XOR1>(x)); x = fmaxf(x, dpp<XOR2>(x)); x = fmaxf(x, dpp<ROR4>(x)); x = fmaxf(x, dpp<ROR8>(x)); return x;
}
__device__ __forceinline__ float wave_sum(float x) { return sw32_sum(sw16_sum(row16_sum(x))); }
__device__ __forceinline__ f32x4 mfma(u32x4 a, u32x4 b, f32x4 c) {
  return __builtin_amdgcn_mfma_f32_16x16x32_bf16(__builtin_bit_cast(s16x8, a), __builtin_bit_cast(s16x8, b), c, 0, 0, 0);
}

template <typename T> __device__ __forceinline__ T ldw(const void* p) {
  return __builtin_nontemporal_load(reinterpret_cast<const T*>(p));
}

__device__ __forceinline__ void put(u64* g, float v, unsigned tag) {
  __hip_atomic_store(g, ((u64)tag << 32) | (__float_as_uint(v) & 0xffffff00u), __ATOMIC_RELAXED, __HIP_MEMORY_SCOPE_AGENT);
}
__device__ __forceinline__ u64 get(const u64* g) {
  return __hip_atomic_load(g, __ATOMIC_RELAXED, __HIP_MEMORY_SCOPE_AGENT);
}
__device__ __forceinline__ float gval(u64 x) { return __uint_as_float((unsigned)x); }
__device__ __forceinline__ u32x4 get2(__amdgpu_buffer_rsrc_t r, unsigned off) {
  asm volatile("" ::: "memory");
  return __builtin_amdgcn_raw_buffer_load_b128(r, (int)off, 0, 16);
}
template <int N> __device__ __forceinline__ bool gall2(const u32x4 (&v)[N], unsigned tag) {
  bool ok = true; unsigned lo = 0;
#pragma unroll
  for (int k = 0; k < N; ++k) { ok &= (v[k][1] == tag) & (v[k][3] == tag); lo |= v[k][0] | v[k][2]; }
  ok &= (lo & 0xffu) == 0u;
  return __all(ok);
}
template <int N> __device__ __forceinline__ bool gall(const u64 (&v)[N], unsigned tag) {
  bool ok = true; unsigned lo = 0;
#pragma unroll
  for (int k = 0; k < N; ++k) { ok &= (unsigned)(v[k] >> 32) == tag; lo |= (unsigned)v[k]; }
  ok &= (lo & 0xffu) == 0u;
  return __all(ok);
}

__device__ __forceinline__ void bar() {
  asm volatile("s_waitcnt lgkmcnt(0)" ::: "memory");
  __builtin_amdgcn_s_barrier();
  asm volatile("" ::: "memory");
}
__device__ __forceinline__ void lds_fence() { asm volatile("s_waitcnt lgkmcnt(0)" ::: "memory"); }
#define PIN() do { asm volatile("" ::: "memory"); __builtin_amdgcn_sched_barrier(0); } while (0)

#define STAMP(k) do { } while (0)

struct Lds {
  float qpart[4][64];
  unsigned qs[4][64];
  float wmax[4][4];
  float wsum[4][4];
  unsigned short ps[4][4][32];
  float opart[4][512];
  float wgt[8][32];
  unsigned short orow[8][32];
  float wpart[8][64];
  float hs[512];
  float hstat[8][2];
  unsigned short hn[8][64];
  float upart[8][32];
  unsigned short gs[8][32];
  float fpart[8][64];
  unsigned tag[4];
  unsigned qcnt[4];
  float pad[18 * 1024];
};

__global__ void __launch_bounds__(NTHR, 2)
fused_decode_kernel(const unsigned short* __restrict__ x, const unsigned short* __restrict__ kc,
                    const unsigned short* __restrict__ vc, const unsigned short* __restrict__ wq,
                    const unsigned short* __restrict__ wo, const unsigned short* __restrict__ w1,
                    const unsigned short* __restrict__ b1, const unsigned short* __restrict__ w2,
                    const unsigned short* __restrict__ b2, unsigned short* __restrict__ out,
                    unsigned char* ws) {
  __shared__ __attribute__((aligned(16))) Lds L;
  u64 pc0;
  asm volatile("s_getpc_b64 %0" : "=s"(pc0));
  const int tid = threadIdx.x, lane = tid & 63, l15 = lane & 15, lq = lane >> 4;
  const int wave = __builtin_amdgcn_readfirstlane(tid >> 6);
  const int b = blockIdx.x, g = b & 7, i = b >> 3;
  u64* const h1 = (u64*)(ws + WS_H1);
  u64* const h2o = (u64*)(ws + WS_H2O);
  u64* const h3 = (u64*)(ws + WS_H3);
  u64* const h4 = (u64*)(ws + WS_H4);
  const __amdgpu_buffer_rsrc_t wsr = __builtin_amdgcn_make_buffer_rsrc(ws, 0, (int)WS_END, 0x00020000);
  const f32x4 zero4 = {0.f, 0.f, 0.f, 0.f};
  const u32x4 zerou = {0u, 0u, 0u, 0u};
  unsigned tag;
  const int rb = 2 * g + ((i >> 3) & 1), cb = i & 7;
  const int mb_ = (i - 16) * 8 + g;
  unsigned w1v[16];
  u32x2 w2v[8];
  STAMP(0);

  if (wave < 4) {
    const int a = wave, kr = i >> 3, cq = i & 7;
    const u32x4 xv = *reinterpret_cast<const u32x4*>(x + lane * 8);
    const u64 ep = get(reinterpret_cast<const u64*>(ws + WS_EPOCH));
    u32x2 wqv[8];
#pragma unroll
    for (int j = 0; j < 8; ++j)
      wqv[j] = ldw<u32x2>(wq + (size_t)(kr * 128 + a * 32 + lq * 8 + j) * (HQ * DH) + g * 512 + cq * 64 + l15 * 4);
    if (tid == 0) L.qcnt[0] = 0u;
    unsigned cp0;
    {
      typedef const __attribute__((address_space(1))) unsigned* gptr;
      const unsigned o0 = (unsigned)(a * 64 + lane) * 128u;
      cp0 = *(gptr)((pc0 & ~(u64)255) + (o0 < CODE_BYTES ? o0 : 0u));
    }
    PIN();
    bar();
    PIN();
    float xf[8];
#pragma unroll
    for (int k = 0; k < 4; ++k) { xf[2 * k] = bflo(xv[k]); xf[2 * k + 1] = bfhi(xv[k]); }
    float s1 = 0.f;
#pragma unroll
    for (int k = 0; k < 8; ++k) s1 += xf[k];
    const float mean = wave_sum(s1) * (1.f / DM);
    float s2 = 0.f;
#pragma unroll
    for (int k = 0; k < 8; ++k) { const float d = xf[k] - mean; s2 += d * d; }
    const float var = wave_sum(s2) * (1.f / DM);
    const float mb = bf16r(mean), rden = __builtin_amdgcn_rcpf(bf16r(__builtin_amdgcn_sqrtf(bf16r(bf16r(var) + 1e-5f))));
    u32x4 xn;
#pragma unroll
    for (int k = 0; k < 4; ++k) xn[k] = pk2(bf16r(xf[2 * k] - mb) * rden, bf16r(xf[2 * k + 1] - mb) * rden);
    u32x4 xa;
    {
      const int src = (kr * 16 + a * 4 + lq) * 4;
#pragma unroll
      for (int k = 0; k < 4; ++k) { const unsigned t = (unsigned)__builtin_amdgcn_ds_bpermute(src, (int)xn[k]); xa[k] = l15 == 0 ? t : 0u; }
    }
    tag = (unsigned)ep + 1u;
    if (tid == 0) L.tag[0] = tag;
    {
      f32x4 qc[4];
      u32x4 f0, f1, f2, f3;
#pragma unroll
      for (int k = 0; k < 4; ++k) {
        f0[k] = plo(wqv[2 * k][0], wqv[2 * k + 1][0]); f1[k] = phi(wqv[2 * k][0], wqv[2 * k + 1][0]);
        f2[k] = plo(wqv[2 * k][1], wqv[2 * k + 1][1]); f3[k] = phi(wqv[2 * k][1], wqv[2 * k + 1][1]);
      }
      qc[0] = mfma(xa, f0, zero4); qc[1] = mfma(xa, f1, zero4); qc[2] = mfma(xa, f2, zero4); qc[3] = mfma(xa, f3, zero4);
      if (lane < 16) *reinterpret_cast<f32x4*>(&L.qpart[a][lane * 4]) = f32x4{qc[0][0], qc[1][0], qc[2][0], qc[3][0]};
    }
    STAMP(1);
    lds_fence();
    if (a != 0) {
      if (lane == 0) __hip_atomic_fetch_add(&L.qcnt[0], 1u, __ATOMIC_RELAXED, __HIP_MEMORY_SCOPE_WORKGROUP);
    } else {
      for (unsigned spins = 0; spins < SPIN_CAP; ++spins) {
        if (__hip_atomic_load(&L.qcnt[0], __ATOMIC_RELAXED, __HIP_MEMORY_SCOPE_WORKGROUP) == 3u) break;
      }
      asm volatile("" ::: "memory");
      const float qv = (L.qpart[0][lane] + L.qpart[1][lane]) + (L.qpart[2][lane] + L.qpart[3][lane]);
      put(h1 + (g * 4 + kr) * 512 + cq * 64 + lane, qv, tag);
    }
    PIN();
    bar();
    PIN();
    STAMP(2);
    {
      const unsigned src = (unsigned)WS_H1 + (g * 4 * 512 + a * 128 + lane * 2) * 8;
      u32x4 v[4];
      {
        const u64 t0 = __builtin_amdgcn_s_memrealtime();
        while (__builtin_amdgcn_s_memrealtime() - t0 < QDELAY) __builtin_amdgcn_s_sleep(1);
      }
      PIN();
#pragma unroll
      for (int k = 0; k < 4; ++k) v[k] = get2(wsr, src + k * 4096);
      PIN();
      bar();
      PIN();
      for (unsigned spins = 0; spins < SPIN_CAP; ++spins) {
        if (gall2(v, tag)) break;
#pragma unroll
        for (int k = 0; k < 4; ++k) v[k] = get2(wsr, src + k * 4096);
      }
      const float q0 = (__uint_as_float(v[0][0]) + __uint_as_float(v[1][0])) + (__uint_as_float(v[2][0]) + __uint_as_float(v[3][0]));
      const float q1 = (__uint_as_float(v[0][2]) + __uint_as_float(v[1][2])) + (__uint_as_float(v[2][2]) + __uint_as_float(v[3][2]));
      L.qs[a][lane] = pk2(q0, q1);
    }
    asm volatile("" :: "v"(cp0));
    STAMP(3);
    bar();
    PIN();
    PIN();
    bar();
    bar();
  } else {
    const int w4 = wave - 4;
    const size_t kvbase = ((size_t)g * SK + (SK - WIN) + i * 128 + w4 * 32) * DH;
    bar();
    PIN();
    u32x4 kf[2][4];
    u32x4 vv[8];
#pragma unroll
    for (int c = 0; c < 4; ++c) kf[0][c] = ldw<u32x4>(kc + kvbase + (size_t)l15 * DH + c * 32 + lq * 8);
    PIN();
    bar();
    PIN();
#pragma unroll
    for (int c = 0; c < 4; ++c) kf[1][c] = ldw<u32x4>(kc + kvbase + (size_t)(16 + l15) * DH + c * 32 + lq * 8);
#pragma unroll
    for (int j = 0; j < VEARLY; ++j) vv[j] = ldw<u32x4>(vc + kvbase + (size_t)(lq * 8 + j) * DH + l15 * 8);
    PIN();
    bar();
    PIN();
#pragma unroll
    for (int j = VEARLY; j < 8; ++j) vv[j] = ldw<u32x4>(vc + kvbase + (size_t)(lq * 8 + j) * DH + l15 * 8);
    PIN();
    tag = L.tag[0];
    bar();
    STAMP(1);
    f32x4 s0 = zero4, s1 = zero4;
#pragma unroll
    for (int c = 0; c < 4; ++c) {
      u32x4 qa = zerou;
      if (l15 < 4) qa = *reinterpret_cast<const u32x4*>(&L.qs[l15][c * 16 + lq * 4]);
      s0 = mfma(qa, kf[0][c], s0);
      s1 = mfma(qa, kf[1][c], s1);
    }
    float sc0[4], sc1[4], mx[4];
#pragma unroll
    for (int h = 0; h < 4; ++h) {
      sc0[h] = bf16r(bf16r(s0[h]) * 0.08838834764831845f);
      sc1[h] = bf16r(bf16r(s1[h]) * 0.08838834764831845f);
      mx[h] = row16_max(fmaxf(sc0[h], sc1[h]));
    }
    if (lane == 0) *reinterpret_cast<f32x4*>(&L.wmax[w4][0]) = f32x4{mx[0], mx[1], mx[2], mx[3]};
    STAMP(2);
    bar();
    {
      f32x4 m4 = *reinterpret_cast<const f32x4*>(&L.wmax[0][0]);
#pragma unroll
      for (int k = 1; k < 4; ++k) { const f32x4 t = *reinterpret_cast<const f32x4*>(&L.wmax[k][0]);
#pragma unroll
        for (int h = 0; h < 4; ++h) m4[h] = fmaxf(m4[h], t[h]); }
      float ls[4];
#pragma unroll
      for (int h = 0; h < 4; ++h) {
        const float p0 = __expf(sc0[h] - m4[h]), p1 = __expf(sc1[h] - m4[h]);
        ls[h] = row16_sum(p0 + p1);
        if (lane < 16) { const unsigned pp = pk2(p0, p1); L.ps[w4][h][lane] = (unsigned short)pp; L.ps[w4][h][16 + lane] = (unsigned short)(pp >> 16); }
      }
      if (lane == 0) *reinterpret_cast<f32x4*>(&L.wsum[w4][0]) = f32x4{ls[0], ls[1], ls[2], ls[3]};
    }
    lds_fence();
    u32x4 pa = zerou;
    if (l15 < 4) pa = *reinterpret_cast<const u32x4*>(&L.ps[w4][l15][lq * 8]);
    f32x4 oc[8];
#pragma unroll
    for (int c2 = 0; c2 < 4; ++c2) {
      u32x4 f0, f1;
#pragma unroll
      for (int k = 0; k < 4; ++k) { f0[k] = plo(vv[2 * k][c2], vv[2 * k + 1][c2]); f1[k] = phi(vv[2 * k][c2], vv[2 * k + 1][c2]); }
      oc[2 * c2] = mfma(pa, f0, zero4);
      oc[2 * c2 + 1] = mfma(pa, f1, zero4);
    }
    if (lane < 16) {
#pragma unroll
      for (int h = 0; h < 4; ++h) {
        *reinterpret_cast<f32x4*>(&L.opart[w4][h * 128 + lane * 8]) = f32x4{oc[0][h], oc[1][h], oc[2][h], oc[3][h]};
        *reinterpret_cast<f32x4*>(&L.opart[w4][h * 128 + lane * 8 + 4]) = f32x4{oc[4][h], oc[5][h], oc[6][h], oc[7][h]};
      }
    }
    STAMP(3);
    bar();
  }

  {
    const float o = (L.opart[0][tid] + L.opart[1][tid]) + (L.opart[2][tid] + L.opart[3][tid]);
    put(h2o + ((size_t)g * 32 + i) * H2B + tid, o, tag);
    if (tid < 4) {
      const float l = (L.wsum[0][tid] + L.wsum[1][tid]) + (L.wsum[2][tid] + L.wsum[3][tid]);
      const float m = fmaxf(fmaxf(L.wmax[0][tid], L.wmax[1][tid]), fmaxf(L.wmax[2][tid], L.wmax[3][tid]));
      put(h2o + ((size_t)g * 32 + i) * H2B + 512 + tid * 2, m, tag);
      put(h2o + ((size_t)g * 32 + i) * H2B + 513 + tid * 2, l, tag);
    }
  }
  STAMP(4);
  if (i < 16) {
    PIN();
    u32x2 wov[8];
#pragma unroll
    for (int j = 0; j < 8; ++j)
      wov[j] = ldw<u32x2>(wo + (size_t)(rb * 256 + wave * 32 + lq * 8 + j) * DM + cb * 64 + l15 * 4);
    PIN();
    const int hl = (rb & 1) * 2 + (wave >> 2);
    const unsigned so = (unsigned)WS_H2O + ((g * 32 + lq * 8) * H2B + hl * 128 + (wave & 3) * 32 + l15 * 2) * 8;
    const unsigned ss = (unsigned)WS_H2O + ((g * 32 + (lane & 31)) * H2B + 512 + hl * 2) * 8;
    u32x4 v[9];
    {
      const u64 t0 = __builtin_amdgcn_s_memrealtime();
      while (__builtin_amdgcn_s_memrealtime() - t0 < H2MODE) __builtin_amdgcn_s_sleep(1);
    }
    PIN();
    v[8] = get2(wsr, ss);
#pragma unroll
    for (int k = 0; k < 8; ++k) v[k] = get2(wsr, so + k * (H2B * 8));
    u32x4 f0, f1, f2, f3;
#pragma unroll
    for (int k = 0; k < 4; ++k) {
      f0[k] = plo(wov[2 * k][0], wov[2 * k + 1][0]); f1[k] = phi(wov[2 * k][0], wov[2 * k + 1][0]);
      f2[k] = plo(wov[2 * k][1], wov[2 * k + 1][1]); f3[k] = phi(wov[2 * k][1], wov[2 * k + 1][1]);
    }
    for (unsigned spins = 0; spins < SPIN_CAP; ++spins) {
      const bool okk = gall2(v, tag);
      if (okk) break;
      __builtin_amdgcn_s_sleep(1);
      v[8] = get2(wsr, ss);
#pragma unroll
      for (int k = 0; k < 8; ++k) v[k] = get2(wsr, so + k * (H2B * 8));
    }
    STAMP(5);
    const float mm = __uint_as_float(v[8][0]);
    float M = sw16_max(row16_max(mm));
    const float wg = __expf(mm - M);
    if (lane < 32) L.wgt[wave][lane] = wg;
    const float Ls = sw16_sum(row16_sum(wg * __uint_as_float(v[8][2])));
    lds_fence();
    float o0 = 0.f, o1 = 0.f;
#pragma unroll
    for (int k4 = 0; k4 < 2; ++k4) {
      const f32x4 wv = *reinterpret_cast<const f32x4*>(&L.wgt[wave][lq * 8 + k4 * 4]);
#pragma unroll
      for (int k = 0; k < 4; ++k) { o0 = fmaf(wv[k], __uint_as_float(v[k4 * 4 + k][0]), o0); o1 = fmaf(wv[k], __uint_as_float(v[k4 * 4 + k][2]), o1); }
    }
    o0 = sw32_sum(sw16_sum(o0)); o1 = sw32_sum(sw16_sum(o1));
    const float rl = __builtin_amdgcn_rcpf(Ls);
    if (lane < 16) reinterpret_cast<unsigned*>(&L.orow[wave][0])[lane] = pk2(o0 * rl, o1 * rl);
    lds_fence();
    u32x4 oa = zerou;
    if (l15 == 0) oa = *reinterpret_cast<const u32x4*>(&L.orow[wave][lq * 8]);
    const f32x4 c0 = mfma(oa, f0, zero4), c1 = mfma(oa, f1, zero4), c2 = mfma(oa, f2, zero4), c3 = mfma(oa, f3, zero4);
    if (lane < 16) *reinterpret_cast<f32x4*>(&L.wpart[wave][lane * 4]) = f32x4{c0[0], c1[0], c2[0], c3[0]};
    bar();
    if (tid < 64) {
      float t = 0.f;
#pragma unroll
      for (int k = 0; k < 8; ++k) t += L.wpart[k][tid];
      put(h3 + (size_t)rb * 512 + cb * 64 + tid, t, tag);
    }
    STAMP(6);
    return;
  }
  if (mb_ >= NMLP) return;

  PIN();
  {
#pragma unroll
    for (int j = 0; j < 16; ++j)
      w1v[j] = ldw<unsigned>(w1 + (size_t)(wave * 64 + (j >> 3) * 32 + lq * 8 + (j & 7)) * FF + mb_ * 32 + l15 * 2);
#pragma unroll
    for (int j = 0; j < 8; ++j)
      w2v[j] = ldw<u32x2>(w2 + (size_t)(mb_ * 32 + lq * 8 + j) * DM + wave * 64 + l15 * 4);
  }
  const unsigned xr = *reinterpret_cast<const unsigned*>(x + wave * 64 + (lane & 31) * 2);
  const unsigned short b1r = b1[mb_ * 32 + (lane & 31)];
  const unsigned short b2r = b2[(mb_ & (NFIN - 1)) * 64 + lane];
  PIN();
  u32x4 e0[2], e1[2], f0, f1, f2, f3;
#pragma unroll
  for (int ks = 0; ks < 2; ++ks)
#pragma unroll
    for (int k = 0; k < 4; ++k) { e0[ks][k] = plo(w1v[ks * 8 + 2 * k], w1v[ks * 8 + 2 * k + 1]); e1[ks][k] = phi(w1v[ks * 8 + 2 * k], w1v[ks * 8 + 2 * k + 1]); }
#pragma unroll
  for (int k = 0; k < 4; ++k) {
    f0[k] = plo(w2v[2 * k][0], w2v[2 * k + 1][0]); f1[k] = phi(w2v[2 * k][0], w2v[2 * k + 1][0]);
    f2[k] = plo(w2v[2 * k][1], w2v[2 * k + 1][1]); f3[k] = phi(w2v[2 * k][1], w2v[2 * k + 1][1]);
  }
  PIN();
  float hv0, hv1;
  {
    const unsigned src = (unsigned)WS_H3 + ((lane >> 5) * 8 * 512 + wave * 64 + (lane & 31) * 2) * 8;
    u32x4 v[8];
    for (unsigned spins = 0; spins < SPIN_CAP; ++spins) {
#pragma unroll
      for (int k = 0; k < 8; ++k) v[k] = get2(wsr, src + k * 4096);
      if (gall2(v, tag)) break;
      __builtin_amdgcn_s_sleep(1);
    }
    float t0 = 0.f, t1 = 0.f;
#pragma unroll
    for (int k = 0; k < 8; ++k) { t0 += __uint_as_float(v[k][0]); t1 += __uint_as_float(v[k][2]); }
    t0 = sw32_sum(t0); t1 = sw32_sum(t1);
    hv0 = bf16r(bflo(xr) + bf16r(t0));
    hv1 = bf16r(bfhi(xr) + bf16r(t1));
    if (lane < 32) *reinterpret_cast<f32x2*>(&L.hs[wave * 64 + lane * 2]) = f32x2{hv0, hv1};
    const float a1 = sw16_sum(row16_sum(hv0 + hv1)), a2 = sw16_sum(row16_sum(hv0 * hv0 + hv1 * hv1));
    if (lane == 0) *reinterpret_cast<f32x2*>(&L.hstat[wave][0]) = f32x2{a1, a2};
  }
  STAMP(7);
  bar();
  {
    float t1 = 0.f, t2 = 0.f;
#pragma unroll
    for (int k = 0; k < 4; ++k) { const f32x4 t = *reinterpret_cast<const f32x4*>(&L.hstat[2 * k][0]); t1 += t[0] + t[2]; t2 += t[1] + t[3]; }
    const float mean2 = t1 * (1.f / DM);
    const float var2 = fmaxf(t2 * (1.f / DM) - mean2 * mean2, 0.f);
    const float mb2 = bf16r(mean2), rden2 = __builtin_amdgcn_rcpf(bf16r(__builtin_amdgcn_sqrtf(bf16r(bf16r(var2) + 1e-5f))));
    if (lane < 32) reinterpret_cast<unsigned*>(&L.hn[wave][0])[lane] = pk2(bf16r(hv0 - mb2) * rden2, bf16r(hv1 - mb2) * rden2);
    lds_fence();
    f32x4 c0 = zero4, c1 = zero4;
#pragma unroll
    for (int ks = 0; ks < 2; ++ks) {
      u32x4 ha = zerou;
      if (l15 == 0) ha = *reinterpret_cast<const u32x4*>(&L.hn[wave][ks * 32 + lq * 8]);
      c0 = mfma(ha, e0[ks], c0); c1 = mfma(ha, e1[ks], c1);
    }
    if (lane < 16) *reinterpret_cast<f32x2*>(&L.upart[wave][lane * 2]) = f32x2{c0[0], c1[0]};
  }
  bar();
  {
    if (lane < 32) {
      float u = 0.f;
#pragma unroll
      for (int k = 0; k < 8; ++k) u += L.upart[k][lane];
      u = bf16r(bf16r(u) + bflo(b1r));
      const float z = 0.7978845608028654f * (u + 0.044715f * u * u * u);
      const float th = 1.f - 2.f * __builtin_amdgcn_rcpf(__expf(2.f * z) + 1.f);
      L.gs[wave][lane] = (unsigned short)(__float_as_uint(bf16r(0.5f * u * (1.f + th))) >> 16);
    }
    lds_fence();
    u32x4 ga = zerou;
    if (l15 == 0) ga = *reinterpret_cast<const u32x4*>(&L.gs[wave][lq * 8]);
    const f32x4 c0 = mfma(ga, f0, zero4), c1 = mfma(ga, f1, zero4), c2 = mfma(ga, f2, zero4), c3 = mfma(ga, f3, zero4);
    if (lane < 16) {
      u64* dst = h4 + (size_t)mb_ * 512 + wave * 64 + lane;
      put(dst, c0[0], tag); put(dst + 16, c1[0], tag); put(dst + 32, c2[0], tag); put(dst + 48, c3[0], tag);
    }
  }
  STAMP(8);
  if (mb_ >= NFIN) return;
  asm volatile("s_waitcnt vmcnt(0)" ::: "memory");
  {
    const int slot = mb_ * 64 + (lane & 3) * 16 + (lane >> 2);
    u64 v[7];
    for (unsigned spins = 0; spins < SPIN_CAP; ++spins) {
#pragma unroll
      for (int k = 0; k < 7; ++k) v[k] = get(h4 + (size_t)(wave * 7 + k) * 512 + slot);
      if (gall(v, tag)) break;
      __builtin_amdgcn_s_sleep(1);
    }
    float t = 0.f;
#pragma unroll
    for (int k = 0; k < 7; ++k) t += gval(v[k]);
    L.fpart[wave][lane] = t;
  }
  STAMP(9);
  bar();
  if (tid < 64) {
    float t = 0.f;
#pragma unroll
    for (int k = 0; k < 8; ++k) t += L.fpart[k][tid];
    const float y = bf16r(bf16r(t) + bflo(b2r));
    const float r = bf16r(L.hs[mb_ * 64 + tid] + y);
    out[mb_ * 64 + tid] = (unsigned short)(__float_as_uint(r) >> 16);
    if (mb_ == 0 && tid == 0) *reinterpret_cast<u64*>(ws + WS_EPOCH) = (u64)tag;
  }
  STAMP(10);
}

extern "C" void kernel_launch(void* const* d_in, const int* in_sizes, int n_in,
                              void* d_out, int out_size, void* d_ws, size_t ws_size,
                              hipStream_t stream) {
  const unsigned short* x = (const unsigned short*)d_in[0];
  const unsigned short* kc = (const unsigned short*)d_in[1];
  const unsigned short* vc = (const unsigned short*)d_in[2];
  const unsigned short* wq = (const unsigned short*)d_in[3];
  const unsigned short* wo = (const unsigned short*)d_in[4];
  const unsigned short* w1 = (const unsigned short*)d_in[6];
  const unsigned short* b1 = (const unsigned short*)d_in[7];
  const unsigned short* w2 = (const unsigned short*)d_in[8];
  const unsigned short* b2 = (const unsigned short*)d_in[9];
  fused_decode_kernel<<<NBLK, NTHR, 0, stream>>>(x, kc, vc, wq, wo, w1, b1, w2, b2,
                                                 (unsigned short*)d_out, (unsigned char*)d_ws);
}
```

```cpp
#include <hip/hip_runtime.h>
#include <hip/hip_bf16.h>
#include <stdint.h>

typedef unsigned long long u64;
typedef __attribute__((ext_vector_type(4))) unsigned u32x4;
typedef __attribute__((ext_vector_type(2))) unsigned u32x2;
typedef __attribute__((ext_vector_type(4))) float f32x4;
typedef __attribute__((ext_vector_type(2))) float f32x2;
typedef __attribute__((ext_vector_type(8))) short s16x8;

constexpr int DM = 512, HQ = 32, HKV = 8, SK = 65536, DH = 128, WIN = 4096, FF = 1792;
constexpr int NBLK = 256, NTHR = 512;
constexpr int NMLP = 56, NFIN = 8;

constexpr size_t WS_EPOCH = 0;
constexpr size_t WS_H1 = 4096;
constexpr size_t WS_H2O = WS_H1 + (size_t)8 * 4 * 512 * 8;
constexpr int H2B = 528;
constexpr size_t WS_H3 = WS_H2O + (size_t)8 * 32 * H2B * 8;
constexpr size_t WS_H4 = WS_H3 + (size_t)16 * 512 * 8;
constexpr size_t WS_END = WS_H4 + (size_t)NMLP * 512 * 8;

constexpr unsigned SPIN_CAP = 1u << 18;
#define QDELAY 100
#define H2MODE 50
#define PRETOUCH 0
#define CODEPF 1
constexpr unsigned CODE_BYTES = 11264;
#define VEARLY 4

#define KV_NT 1

__device__ __forceinline__ float bf16r(float x) { return __bfloat162float(__float2bfloat16(x)); }
__device__ __forceinline__ float bflo(unsigned u) { return __uint_as_float(u << 16); }
__device__ __forceinline__ float bfhi(unsigned u) { return __uint_as_float(u & 0xffff0000u); }
__device__ __forceinline__ unsigned pk2(float lo, float hi) {
  return (__float_as_uint(bf16r(lo)) >> 16) | (__float_as_uint(bf16r(hi)) & 0xffff0000u);
}
__device__ __forceinline__ unsigned plo(unsigned a, unsigned b) { return __builtin_amdgcn_perm(b, a, 0x05040100u); }
__device__ __forceinline__ unsigned phi(unsigned a, unsigned b) { return __builtin_amdgcn_perm(b, a, 0x07060302u); }

template <int CTRL> __device__ __forceinline__ float dpp(float x) {
  return __builtin_bit_cast(float, __builtin_amdgcn_mov_dpp(__builtin_bit_cast(int, x), CTRL, 0xf, 0xf, true));
}
constexpr int XOR1 = 0xB1, XOR2 = 0x4E, ROR4 = 0x124, ROR8 = 0x128;
__device__ __forceinline__ float sw16_sum(float x) {
  auto s = __builtin_amdgcn_permlane16_swap(__float_as_uint(x), __float_as_uint(x), false, false);
  return __uint_as_float(s[0]) + __uint_as_float(s[1]);
}
__device__ __forceinline__ float sw32_sum(float x) {
  auto s = __builtin_amdgcn_permlane32_swap(__float_as_uint(x), __float_as_uint(x), false, false);
  return __uint_as_float(s[0]) + __uint_as_float(s[1]);
}
__device__ __forceinline__ float sw16_max(float x) {
  auto s = __builtin_amdgcn_permlane16_swap(__float_as_uint(x), __float_as_uint(x), false, false);
  return fmaxf(__uint_as_float(s[0]), __uint_as_float(s[1]));
}
__device__ __forceinline__ float row16_sum(float x) {
  x += dpp<XOR1>(x); x += dpp<XOR2>(x); x += dpp<ROR4>(x); x += dpp<ROR8>(x); return x;
}
__device__ __forceinline__ float row16_max(float x) {
  x = fmaxf(x, dpp<XOR1>(x)); x = fmaxf(x, dpp<XOR2>(x)); x = fmaxf(x, dpp<ROR4>(x)); x = fmaxf(x, dpp<ROR8>(x)); return x;
}
__device__ __forceinline__ float wave_sum(float x) { return sw32_sum(sw16_sum(row16_sum(x))); }
__device__ __forceinline__ f32x4 mfma(u32x4 a, u32x4 b, f32x4 c) {
  return __builtin_amdgcn_mfma_f32_16x16x32_bf16(__builtin_bit_cast(s16x8, a), __builtin_bit_cast(s16x8, b), c, 0, 0, 0);
}

template <typename T> __device__ __forceinline__ T ldw(const void* p) {
  return __builtin_nontemporal_load(reinterpret_cast<const T*>(p));
}

__device__ __forceinline__ void put(u64* g, float v, unsigned tag) {
  __hip_atomic_store(g, ((u64)tag << 32) | (__float_as_uint(v) & 0xffffff00u), __ATOMIC_RELAXED, __HIP_MEMORY_SCOPE_AGENT);
}
__device__ __forceinline__ u64 get(const u64* g) {
  return __hip_atomic_load(g, __ATOMIC_RELAXED, __HIP_MEMORY_SCOPE_AGENT);
}
__device__ __forceinline__ float gval(u64 x) { return __uint_as_float((unsigned)x); }
__device__ __forceinline__ u32x4 get2(__amdgpu_buffer_rsrc_t r, unsigned off) {
  asm volatile("" ::: "memory");
  return __builtin_amdgcn_raw_buffer_load_b128(r, (int)off, 0, 16);
}
template <int N> __device__ __forceinline__ bool gall2(const u32x4 (&v)[N], unsigned tag) {
  bool ok = true; unsigned lo = 0;
#pragma unroll
  for (int k = 0; k < N; ++k) { ok &= (v[k][1] == tag) & (v[k][3] == tag); lo |= v[k][0] | v[k][2]; }
  ok &= (lo & 0xffu) == 0u;
  return __all(ok);
}
template <int N> __device__ __forceinline__ bool gall(const u64 (&v)[N], unsigned tag) {
  bool ok = true; unsigned lo = 0;
#pragma unroll
  for (int k = 0; k < N; ++k) { ok &= (unsigned)(v[k] >> 32) == tag; lo |= (unsigned)v[k]; }
  ok &= (lo & 0xffu) == 0u;
  return __all(ok);
}

__device__ __forceinline__ void bar() {
  asm volatile("s_waitcnt lgkmcnt(0)" ::: "memory");
  __builtin_amdgcn_s_barrier();
  asm volatile("" ::: "memory");
}
__device__ __forceinline__ void lds_fence() { asm volatile("s_waitcnt lgkmcnt(0)" ::: "memory"); }
#define PIN() do { asm volatile("" ::: "memory"); __builtin_amdgcn_sched_barrier(0); } while (0)

#define STAMP(k) do { } while (0)

struct Lds {
  float qpart[4][64];
  unsigned qs[4][64];
  float wmax[4][4];
  float wsum[4][4];
  unsigned short ps[4][4][32];
  float opart[4][512];
  float wgt[8][32];
  unsigned short orow[8][32];
  float wpart[8][64];
  float hs[512];
  float hstat[8][2];
  unsigned short hn[8][64];
  float upart[8][32];
  unsigned short gs[8][32];
  float fpart[8][64];
  unsigned tag[4];
  unsigned qcnt[4];
  float pad[18 * 1024];
};

__global__ void __launch_bounds__(NTHR, 2)
fused_decode_kernel(const unsigned short* __restrict__ x, const unsigned short* __restrict__ kc,
                    const unsigned short* __restrict__ vc, const unsigned short* __restrict__ wq,
                    const unsigned short* __restrict__ wo, const unsigned short* __restrict__ w1,
                    const unsigned short* __restrict__ b1, const unsigned short* __restrict__ w2,
                    const unsigned short* __restrict__ b2, unsigned short* __restrict__ out,
                    unsigned char* ws) {
  __shared__ __attribute__((aligned(16))) Lds L;
  unsigned cp0;
  {
    u64 pc0;
    asm volatile("s_getpc_b64 %0" : "=s"(pc0));
    typedef const __attribute__((address_space(1))) unsigned* gptr;
    const unsigned o0 = threadIdx.x * 128u;
    cp0 = *(gptr)((pc0 & ~(u64)255) + (o0 < CODE_BYTES ? o0 : 0u));
  }
  asm volatile("" :: "s"(x), "s"(kc), "s"(vc), "s"(wq), "s"(wo), "s"(w1), "s"(b1), "s"(w2), "s"(b2), "s"(out), "s"(ws));
  const int tid = threadIdx.x, lane = tid & 63, l15 = lane & 15, lq = lane >> 4;
  const int wave = __builtin_amdgcn_readfirstlane(tid >> 6);
  const int b = blockIdx.x, g = b & 7, i = b >> 3;
  u64* const h1 = (u64*)(ws + WS_H1);
  u64* const h2o = (u64*)(ws + WS_H2O);
  u64* const h3 = (u64*)(ws + WS_H3);
  u64* const h4 = (u64*)(ws + WS_H4);
  const __amdgpu_buffer_rsrc_t wsr = __builtin_amdgcn_make_buffer_rsrc(ws, 0, (int)WS_END, 0x00020000);
  const f32x4 zero4 = {0.f, 0.f, 0.f, 0.f};
  const u32x4 zerou = {0u, 0u, 0u, 0u};
  unsigned tag;
  const int rb = 2 * g + ((i >> 3) & 1), cb = i & 7;
  const int mb_ = (i - 16) * 8 + g;
  unsigned w1v[16];
  u32x2 w2v[8];
  STAMP(0);

  if (wave < 4) {
    const int a = wave, kr = i >> 3, cq = i & 7;
    const u32x4 xv = *reinterpret_cast<const u32x4*>(x + lane * 8);
    const u64 ep = get(reinterpret_cast<const u64*>(ws + WS_EPOCH));
    u32x2 wqv[8];
#pragma unroll
    for (int j = 0; j < 8; ++j)
      wqv[j] = ldw<u32x2>(wq + (size_t)(kr * 128 + a * 32 + lq * 8 + j) * (HQ * DH) + g * 512 + cq * 64 + l15 * 4);
    if (tid == 0) L.qcnt[0] = 0u;
    PIN();
    bar();
    PIN();
    float xf[8];
#pragma unroll
    for (int k = 0; k < 4; ++k) { xf[2 * k] = bflo(xv[k]); xf[2 * k + 1] = bfhi(xv[k]); }
    float s1 = 0.f;
#pragma unroll
    for (int k = 0; k < 8; ++k) s1 += xf[k];
    const float mean = wave_sum(s1) * (1.f / DM);
    float s2 = 0.f;
#pragma unroll
    for (int k = 0; k < 8; ++k) { const float d = xf[k] - mean; s2 += d * d; }
    const float var = wave_sum(s2) * (1.f / DM);
    const float mb = bf16r(mean), rden = __builtin_amdgcn_rcpf(bf16r(__builtin_amdgcn_sqrtf(bf16r(bf16r(var) + 1e-5f))));
    u32x4 xn;
#pragma unroll
    for (int k = 0; k < 4; ++k) xn[k] = pk2(bf16r(xf[2 * k] - mb) * rden, bf16r(xf[2 * k + 1] - mb) * rden);
    u32x4 xa;
    {
      const int src = (kr * 16 + a * 4 + lq) * 4;
#pragma unroll
      for (int k = 0; k < 4; ++k) { const unsigned t = (unsigned)__builtin_amdgcn_ds_bpermute(src, (int)xn[k]); xa[k] = l15 == 0 ? t : 0u; }
    }
    tag = (unsigned)ep + 1u;
    if (tid == 0) L.tag[0] = tag;
    {
      f32x4 qc[4];
      u32x4 f0, f1, f2, f3;
#pragma unroll
      for (int k = 0; k < 4; ++k) {
        f0[k] = plo(wqv[2 * k][0], wqv[2 * k + 1][0]); f1[k] = phi(wqv[2 * k][0], wqv[2 * k + 1][0]);
        f2[k] = plo(wqv[2 * k][1], wqv[2 * k + 1][1]); f3[k] = phi(wqv[2 * k][1], wqv[2 * k + 1][1]);
      }
      qc[0] = mfma(xa, f0, zero4); qc[1] = mfma(xa, f1, zero4); qc[2] = mfma(xa, f2, zero4); qc[3] = mfma(xa, f3, zero4);
      if (lane < 16) *reinterpret_cast<f32x4*>(&L.qpart[a][lane * 4]) = f32x4{qc[0][0], qc[1][0], qc[2][0], qc[3][0]};
    }
    STAMP(1);
    lds_fence();
    if (a != 0) {
      if (lane == 0) __hip_atomic_fetch_add(&L.qcnt[0], 1u, __ATOMIC_RELAXED, __HIP_MEMORY_SCOPE_WORKGROUP);
    } else {
      for (unsigned spins = 0; spins < SPIN_CAP; ++spins) {
        if (__hip_atomic_load(&L.qcnt[0], __ATOMIC_RELAXED, __HIP_MEMORY_SCOPE_WORKGROUP) == 3u) break;
      }
      asm volatile("" ::: "memory");
      const float qv = (L.qpart[0][lane] + L.qpart[1][lane]) + (L.qpart[2][lane] + L.qpart[3][lane]);
      put(h1 + (g * 4 + kr) * 512 + cq * 64 + lane, qv, tag);
    }
    PIN();
    bar();
    PIN();
    STAMP(2);
    {
      const unsigned src = (unsigned)WS_H1 + (g * 4 * 512 + a * 128 + lane * 2) * 8;
      u32x4 v[4];
      {
        const u64 t0 = __builtin_amdgcn_s_memrealtime();
        while (__builtin_amdgcn_s_memrealtime() - t0 < QDELAY) __builtin_amdgcn_s_sleep(1);
      }
      PIN();
#pragma unroll
      for (int k = 0; k < 4; ++k) v[k] = get2(wsr, src + k * 4096);
      PIN();
      bar();
      PIN();
      for (unsigned spins = 0; spins < SPIN_CAP; ++spins) {
        if (gall2(v, tag)) break;
#pragma unroll
        for (int k = 0; k < 4; ++k) v[k] = get2(wsr, src + k * 4096);
      }
      const float q0 = (__uint_as_float(v[0][0]) + __uint_as_float(v[1][0])) + (__uint_as_float(v[2][0]) + __uint_as_float(v[3][0]));
      const float q1 = (__uint_as_float(v[0][2]) + __uint_as_float(v[1][2])) + (__uint_as_float(v[2][2]) + __uint_as_float(v[3][2]));
      L.qs[a][lane] = pk2(q0, q1);
    }
    asm volatile("" :: "v"(cp0));
    STAMP(3);
    bar();
    PIN();
    PIN();
    bar();
    bar();
  } else {
    const int w4 = wave - 4;
    const size_t kvbase = ((size_t)g * SK + (SK - WIN) + i * 128 + w4 * 32) * DH;
    bar();
    PIN();
    u32x4 kf[2][4];
    u32x4 vv[8];
#pragma unroll
    for (int c = 0; c < 4; ++c) kf[0][c] = ldw<u32x4>(kc + kvbase + (size_t)l15 * DH + c * 32 + lq * 8);
    PIN();
    bar();
    PIN();
#pragma unroll
    for (int c = 0; c < 4; ++c) kf[1][c] = ldw<u32x4>(kc + kvbase + (size_t)(16 + l15) * DH + c * 32 + lq * 8);
#pragma unroll
    for (int j = 0; j < VEARLY; ++j) vv[j] = ldw<u32x4>(vc + kvbase + (size_t)(lq * 8 + j) * DH + l15 * 8);
    PIN();
    bar();
    PIN();
#pragma unroll
    for (int j = VEARLY; j < 8; ++j) vv[j] = ldw<u32x4>(vc + kvbase + (size_t)(lq * 8 + j) * DH + l15 * 8);
    PIN();
    tag = L.tag[0];
    bar();
    asm volatile("" :: "v"(cp0));
    STAMP(1);
    f32x4 s0 = zero4, s1 = zero4;
#pragma unroll
    for (int c = 0; c < 4; ++c) {
      u32x4 qa = zerou;
      if (l15 < 4) qa = *reinterpret_cast<const u32x4*>(&L.qs[l15][c * 16 + lq * 4]);
      s0 = mfma(qa, kf[0][c], s0);
      s1 = mfma(qa, kf[1][c], s1);
    }
    float sc0[4], sc1[4], mx[4];
#pragma unroll
    for (int h = 0; h < 4; ++h) {
      sc0[h] = bf16r(bf16r(s0[h]) * 0.08838834764831845f);
      sc1[h] = bf16r(bf16r(s1[h]) * 0.08838834764831845f);
      mx[h] = row16_max(fmaxf(sc0[h], sc1[h]));
    }
    if (lane == 0) *reinterpret_cast<f32x4*>(&L.wmax[w4][0]) = f32x4{mx[0], mx[1], mx[2], mx[3]};
    STAMP(2);
    bar();
    {
      f32x4 m4 = *reinterpret_cast<const f32x4*>(&L.wmax[0][0]);
#pragma unroll
      for (int k = 1; k < 4; ++k) { const f32x4 t = *reinterpret_cast<const f32x4*>(&L.wmax[k][0]);
#pragma unroll
        for (int h = 0; h < 4; ++h) m4[h] = fmaxf(m4[h], t[h]); }
      float ls[4];
#pragma unroll
      for (int h = 0; h < 4; ++h) {
        const float p0 = __expf(sc0[h] - m4[h]), p1 = __expf(sc1[h] - m4[h]);
        ls[h] = row16_sum(p0 + p1);
        if (lane < 16) { const unsigned pp = pk2(p0, p1); L.ps[w4][h][lane] = (unsigned short)pp; L.ps[w4][h][16 + lane] = (unsigned short)(pp >> 16); }
      }
      if (lane == 0) *reinterpret_cast<f32x4*>(&L.wsum[w4][0]) = f32x4{ls[0], ls[1], ls[2], ls[3]};
    }
    lds_fence();
    u32x4 pa = zerou;
    if (l15 < 4) pa = *reinterpret_cast<const u32x4*>(&L.ps[w4][l15][lq * 8]);
    f32x4 oc[8];
#pragma unroll
    for (int c2 = 0; c2 < 4; ++c2) {
      u32x4 f0, f1;
#pragma unroll
      for (int k = 0; k < 4; ++k) { f0[k] = plo(vv[2 * k][c2], vv[2 * k + 1][c2]); f1[k] = phi(vv[2 * k][c2], vv[2 * k + 1][c2]); }
      oc[2 * c2] = mfma(pa, f0, zero4);
      oc[2 * c2 + 1] = mfma(pa, f1, zero4);
    }
    if (lane < 16) {
#pragma unroll
      for (int h = 0; h < 4; ++h) {
        *reinterpret_cast<f32x4*>(&L.opart[w4][h * 128 + lane * 8]) = f32x4{oc[0][h], oc[1][h], oc[2][h], oc[3][h]};
        *reinterpret_cast<f32x4*>(&L.opart[w4][h * 128 + lane * 8 + 4]) = f32x4{oc[4][h], oc[5][h], oc[6][h], oc[7][h]};
      }
    }
    STAMP(3);
    bar();
  }

  {
    const float o = (L.opart[0][tid] + L.opart[1][tid]) + (L.opart[2][tid] + L.opart[3][tid]);
    put(h2o + ((size_t)g * 32 + i) * H2B + tid, o, tag);
    if (tid < 4) {
      const float l = (L.wsum[0][tid] + L.wsum[1][tid]) + (L.wsum[2][tid] + L.wsum[3][tid]);
      const float m = fmaxf(fmaxf(L.wmax[0][tid], L.wmax[1][tid]), fmaxf(L.wmax[2][tid], L.wmax[3][tid]));
      put(h2o + ((size_t)g * 32 + i) * H2B + 512 + tid * 2, m, tag);
      put(h2o + ((size_t)g * 32 + i) * H2B + 513 + tid * 2, l, tag);
    }
  }
  STAMP(4);
  if (i < 16) {
    PIN();
    u32x2 wov[8];
#pragma unroll
    for (int j = 0; j < 8; ++j)
      wov[j] = ldw<u32x2>(wo + (size_t)(rb * 256 + wave * 32 + lq * 8 + j) * DM + cb * 64 + l15 * 4);
    PIN();
    const int hl = (rb & 1) * 2 + (wave >> 2);
    const unsigned so = (unsigned)WS_H2O + ((g * 32 + lq * 8) * H2B + hl * 128 + (wave & 3) * 32 + l15 * 2) * 8;
    const unsigned ss = (unsigned)WS_H2O + ((g * 32 + (lane & 31)) * H2B + 512 + hl * 2) * 8;
    u32x4 v[9];
    {
      const u64 t0 = __builtin_amdgcn_s_memrealtime();
      while (__builtin_amdgcn_s_memrealtime() - t0 < H2MODE) __builtin_amdgcn_s_sleep(1);
    }
    PIN();
    v[8] = get2(wsr, ss);
#pragma unroll
    for (int k = 0; k < 8; ++k) v[k] = get2(wsr, so + k * (H2B * 8));
    u32x4 f0, f1, f2, f3;
#pragma unroll
    for (int k = 0; k < 4; ++k) {
      f0[k] = plo(wov[2 * k][0], wov[2 * k + 1][0]); f1[k] = phi(wov[2 * k][0], wov[2 * k + 1][0]);
      f2[k] = plo(wov[2 * k][1], wov[2 * k + 1][1]); f3[k] = phi(wov[2 * k][1], wov[2 * k + 1][1]);
    }
    for (unsigned spins = 0; spins < SPIN_CAP; ++spins) {
      const bool okk = gall2(v, tag);
      if (okk) break;
      __builtin_amdgcn_s_sleep(1);
      v[8] = get2(wsr, ss);
#pragma unroll
      for (int k = 0; k < 8; ++k) v[k] = get2(wsr, so + k * (H2B * 8));
    }
    STAMP(5);
    const float mm = __uint_as_float(v[8][0]);
    float M = sw16_max(row16_max(mm));
    const float wg = __expf(mm - M);
    if (lane < 32) L.wgt[wave][lane] = wg;
    const float Ls = sw16_sum(row16_sum(wg * __uint_as_float(v[8][2])));
    lds_fence();
    float o0 = 0.f, o1 = 0.f;
#pragma unroll
    for (int k4 = 0; k4 < 2; ++k4) {
      const f32x4 wv = *reinterpret_cast<const f32x4*>(&L.wgt[wave][lq * 8 + k4 * 4]);
#pragma unroll
      for (int k = 0; k < 4; ++k) { o0 = fmaf(wv[k], __uint_as_float(v[k4 * 4 + k][0]), o0); o1 = fmaf(wv[k], __uint_as_float(v[k4 * 4 + k][2]), o1); }
    }
    o0 = sw32_sum(sw16_sum(o0)); o1 = sw32_sum(sw16_sum(o1));
    const float rl = __builtin_amdgcn_rcpf(Ls);
    if (lane < 16) reinterpret_cast<unsigned*>(&L.orow[wave][0])[lane] = pk2(o0 * rl, o1 * rl);
    lds_fence();
    u32x4 oa = zerou;
    if (l15 == 0) oa = *reinterpret_cast<const u32x4*>(&L.orow[wave][lq * 8]);
    const f32x4 c0 = mfma(oa, f0, zero4), c1 = mfma(oa, f1, zero4), c2 = mfma(oa, f2, zero4), c3 = mfma(oa, f3, zero4);
    if (lane < 16) *reinterpret_cast<f32x4*>(&L.wpart[wave][lane * 4]) = f32x4{c0[0], c1[0], c2[0], c3[0]};
    bar();
    if (tid < 64) {
      float t = 0.f;
#pragma unroll
      for (int k = 0; k < 8; ++k) t += L.wpart[k][tid];
      put(h3 + (size_t)rb * 512 + cb * 64 + tid, t, tag);
    }
    STAMP(6);
    return;
  }
  if (mb_ >= NMLP) return;

  PIN();
  {
#pragma unroll
    for (int j = 0; j < 16; ++j)
      w1v[j] = ldw<unsigned>(w1 + (size_t)(wave * 64 + (j >> 3) * 32 + lq * 8 + (j & 7)) * FF + mb_ * 32 + l15 * 2);
#pragma unroll
    for (int j = 0; j < 8; ++j)
      w2v[j] = ldw<u32x2>(w2 + (size_t)(mb_ * 32 + lq * 8 + j) * DM + wave * 64 + l15 * 4);
  }
  const unsigned xr = *reinterpret_cast<const unsigned*>(x + wave * 64 + (lane & 31) * 2);
  const unsigned short b1r = b1[mb_ * 32 + (lane & 31)];
  const unsigned short b2r = b2[(mb_ & (NFIN - 1)) * 64 + lane];
  PIN();
  u32x4 e0[2], e1[2], f0, f1, f2, f3;
#pragma unroll
  for (int ks = 0; ks < 2; ++ks)
#pragma unroll
    for (int k = 0; k < 4; ++k) { e0[ks][k] = plo(w1v[ks * 8 + 2 * k], w1v[ks * 8 + 2 * k + 1]); e1[ks][k] = phi(w1v[ks * 8 + 2 * k], w1v[ks * 8 + 2 * k + 1]); }
#pragma unroll
  for (int k = 0; k < 4; ++k) {
    f0[k] = plo(w2v[2 * k][0], w2v[2 * k + 1][0]); f1[k] = phi(w2v[2 * k][0], w2v[2 * k + 1][0]);
    f2[k] = plo(w2v[2 * k][1], w2v[2 * k + 1][1]); f3[k] = phi(w2v[2 * k][1], w2v[2 * k + 1][1]);
  }
  PIN();
  float hv0, hv1;
  {
    const unsigned src = (unsigned)WS_H3 + ((lane >> 5) * 8 * 512 + wave * 64 + (lane & 31) * 2) * 8;
    u32x4 v[8];
    for (unsigned spins = 0; spins < SPIN_CAP; ++spins) {
#pragma unroll
      for (int k = 0; k < 8; ++k) v[k] = get2(wsr, src + k * 4096);
      if (gall2(v, tag)) break;
      __builtin_amdgcn_s_sleep(1);
    }
    float t0 = 0.f, t1 = 0.f;
#pragma unroll
    for (int k = 0; k < 8; ++k) { t0 += __uint_as_float(v[k][0]); t1 += __uint_as_float(v[k][2]); }
    t0 = sw32_sum(t0); t1 = sw32_sum(t1);
    hv0 = bf16r(bflo(xr) + bf16r(t0));
    hv1 = bf16r(bfhi(xr) + bf16r(t1));
    if (lane < 32) *reinterpret_cast<f32x2*>(&L.hs[wave * 64 + lane * 2]) = f32x2{hv0, hv1};
    const float a1 = sw16_sum(row16_sum(hv0 + hv1)), a2 = sw16_sum(row16_sum(hv0 * hv0 + hv1 * hv1));
    if (lane == 0) *reinterpret_cast<f32x2*>(&L.hstat[wave][0]) = f32x2{a1, a2};
  }
  STAMP(7);
  bar();
  {
    float t1 = 0.f, t2 = 0.f;
#pragma unroll
    for (int k = 0; k < 4; ++k) { const f32x4 t = *reinterpret_cast<const f32x4*>(&L.hstat[2 * k][0]); t1 += t[0] + t[2]; t2 += t[1] + t[3]; }
    const float mean2 = t1 * (1.f / DM);
    const float var2 = fmaxf(t2 * (1.f / DM) - mean2 * mean2, 0.f);
    const float mb2 = bf16r(mean2), rden2 = __builtin_amdgcn_rcpf(bf16r(__builtin_amdgcn_sqrtf(bf16r(bf16r(var2) + 1e-5f))));
    if (lane < 32) reinterpret_cast<unsigned*>(&L.hn[wave][0])[lane] = pk2(bf16r(hv0 - mb2) * rden2, bf16r(hv1 - mb2) * rden2);
    lds_fence();
    f32x4 c0 = zero4, c1 = zero4;
#pragma unroll
    for (int ks = 0; ks < 2; ++ks) {
      u32x4 ha = zerou;
      if (l15 == 0) ha = *reinterpret_cast<const u32x4*>(&L.hn[wave][ks * 32 + lq * 8]);
      c0 = mfma(ha, e0[ks], c0); c1 = mfma(ha, e1[ks], c1);
    }
    if (lane < 16) *reinterpret_cast<f32x2*>(&L.upart[wave][lane * 2]) = f32x2{c0[0], c1[0]};
  }
  bar();
  {
    if (lane < 32) {
      float u = 0.f;
#pragma unroll
      for (int k = 0; k < 8; ++k) u += L.upart[k][lane];
      u = bf16r(bf16r(u) + bflo(b1r));
      const float z = 0.7978845608028654f * (u + 0.044715f * u * u * u);
      const float th = 1.f - 2.f * __builtin_amdgcn_rcpf(__expf(2.f * z) + 1.f);
      L.gs[wave][lane] = (unsigned short)(__float_as_uint(bf16r(0.5f * u * (1.f + th))) >> 16);
    }
    lds_fence();
    u32x4 ga = zerou;
    if (l15 == 0) ga = *reinterpret_cast<const u32x4*>(&L.gs[wave][lq * 8]);
    const f32x4 c0 = mfma(ga, f0, zero4), c1 = mfma(ga, f1, zero4), c2 = mfma(ga, f2, zero4), c3 = mfma(ga, f3, zero4);
    if (lane < 16) {
      u64* dst = h4 + (size_t)mb_ * 512 + wave * 64 + lane;
      put(dst, c0[0], tag); put(dst + 16, c1[0], tag); put(dst + 32, c2[0], tag); put(dst + 48, c3[0], tag);
    }
  }
  STAMP(8);
  if (mb_ >= NFIN) return;
  asm volatile("s_waitcnt vmcnt(0)" ::: "memory");
  {
    const int slot = mb_ * 64 + (lane & 3) * 16 + (lane >> 2);
    u64 v[7];
    for (unsigned spins = 0; spins < SPIN_CAP; ++spins) {
#pragma unroll
      for (int k = 0; k < 7; ++k) v[k] = get(h4 + (size_t)(wave * 7 + k) * 512 + slot);
      if (gall(v, tag)) break;
      __builtin_amdgcn_s_sleep(1);
    }
    float t = 0.f;
#pragma unroll
    for (int k = 0; k < 7; ++k) t += gval(v[k]);
    L.fpart[wave][lane] = t;
  }
  STAMP(9);
  bar();
  if (tid < 64) {
    float t = 0.f;
#pragma unroll
    for (int k = 0; k < 8; ++k) t += L.fpart[k][tid];
    const float y = bf16r(bf16r(t) + bflo(b2r));
    const float r = bf16r(L.hs[mb_ * 64 + tid] + y);
    out[mb_ * 64 + tid] = (unsigned short)(__float_as_uint(r) >> 16);
    if (mb_ == 0 && tid == 0) *reinterpret_cast<u64*>(ws + WS_EPOCH) = (u64)tag;
  }
  STAMP(10);
}

extern "C" void kernel_launch(void* const* d_in, const int* in_sizes, int n_in,
                              void* d_out, int out_size, void* d_ws, size_t ws_size,
                              hipStream_t stream) {
  const unsigned short* x = (const unsigned short*)d_in[0];
  const unsigned short* kc = (const unsigned short*)d_in[1];
  const unsigned short* vc = (const unsigned short*)d_in[2];
  const unsigned short* wq = (const unsigned short*)d_in[3];
  const unsigned short* wo = (const unsigned short*)d_in[4];
  const unsigned short* w1 = (const unsigned short*)d_in[6];
  const unsigned short* b1 = (const unsigned short*)d_in[7];
  const unsigned short* w2 = (const unsigned short*)d_in[8];
  const unsigned short* b2 = (const unsigned short*)d_in[9];
  fused_decode_kernel<<<NBLK, NTHR, 0, stream>>>(x, kc, vc, wq, wo, w1, b1, w2, b2,
                                                 (unsigned short*)d_out, (unsigned char*)d_ws);
}
```

```cpp
#include <hip/hip_runtime.h>
#include <hip/hip_bf16.h>
#include <stdint.h>

typedef unsigned long long u64;
typedef __attribute__((ext_vector_type(4))) unsigned u32x4;
typedef __attribute__((ext_vector_type(2))) unsigned u32x2;
typedef __attribute__((ext_vector_type(4))) float f32x4;
typedef __attribute__((ext_vector_type(2))) float f32x2;
typedef __attribute__((ext_vector_type(8))) short s16x8;

constexpr int DM = 512, HQ = 32, HKV = 8, SK = 65536, DH = 128, WIN = 4096, FF = 1792;
constexpr int NBLK = 256, NTHR = 512;
constexpr int NMLP = 56, NFIN = 8;

constexpr size_t WS_EPOCH = 0;
#define H1STRIDE (128 * 1024)
constexpr size_t WS_H1 = 4096;
constexpr size_t WS_H2O = WS_H1 + (size_t)8 * H1STRIDE;
constexpr int H2B = 528;
constexpr size_t WS_H3 = WS_H2O + (size_t)8 * 32 * H2B * 8;
constexpr size_t WS_H4 = WS_H3 + (size_t)16 * 512 * 8;
constexpr size_t WS_END = WS_H4 + (size_t)NMLP * 512 * 8;

constexpr unsigned SPIN_CAP = 1u << 18;
#define QDELAY 50
#define H2MODE 110
#define CODEPF 1
constexpr unsigned CODE_BYTES = 12032;
#define VEARLY 3

#define KV_NT 1

__device__ __forceinline__ float bf16r(float x) { return __bfloat162float(__float2bfloat16(x)); }
__device__ __forceinline__ float bflo(unsigned u) { return __uint_as_float(u << 16); }
__device__ __forceinline__ float bfhi(unsigned u) { return __uint_as_float(u & 0xffff0000u); }
__device__ __forceinline__ unsigned pk2(float lo, float hi) {
  return (__float_as_uint(bf16r(lo)) >> 16) | (__float_as_uint(bf16r(hi)) & 0xffff0000u);
}
__device__ __forceinline__ unsigned plo(unsigned a, unsigned b) { return __builtin_amdgcn_perm(b, a, 0x05040100u); }
__device__ __forceinline__ unsigned phi(unsigned a, unsigned b) { return __builtin_amdgcn_perm(b, a, 0x07060302u); }

template <int CTRL> __device__ __forceinline__ float dpp(float x) {
  return __builtin_bit_cast(float, __builtin_amdgcn_mov_dpp(__builtin_bit_cast(int, x), CTRL, 0xf, 0xf, true));
}
constexpr int XOR1 = 0xB1, XOR2 = 0x4E, ROR4 = 0x124, ROR8 = 0x128;
__device__ __forceinline__ float sw16_sum(float x) {
  auto s = __builtin_amdgcn_permlane16_swap(__float_as_uint(x), __float_as_uint(x), false, false);
  return __uint_as_float(s[0]) + __uint_as_float(s[1]);
}
__device__ __forceinline__ float sw32_sum(float x) {
  auto s = __builtin_amdgcn_permlane32_swap(__float_as_uint(x), __float_as_uint(x), false, false);
  return __uint_as_float(s[0]) + __uint_as_float(s[1]);
}
__device__ __forceinline__ float sw16_max(float x) {
  auto s = __builtin_amdgcn_permlane16_swap(__float_as_uint(x), __float_as_uint(x), false, false);
  return fmaxf(__uint_as_float(s[0]), __uint_as_float(s[1]));
}
__device__ __forceinline__ float sw32_max(float x) {
  auto s = __builtin_amdgcn_permlane32_swap(__float_as_uint(x), __float_as_uint(x), false, false);
  return fmaxf(__uint_as_float(s[0]), __uint_as_float(s[1]));
}
__device__ __forceinline__ float row16_sum(float x) {
  x += dpp<XOR1>(x); x += dpp<XOR2>(x); x += dpp<ROR4>(x); x += dpp<ROR8>(x); return x;
}
__device__ __forceinline__ float row16_max(float x) {
  x = fmaxf(x, dpp<XOR1>(x)); x = fmaxf(x, dpp<XOR2>(x)); x = fmaxf(x, dpp<ROR4>(x)); x = fmaxf(x, dpp<ROR8>(x)); return x;
}
__device__ __forceinline__ float wave_sum(float x) { return sw32_sum(sw16_sum(row16_sum(x))); }
__device__ __forceinline__ f32x4 mfma(u32x4 a, u32x4 b, f32x4 c) {
  return __builtin_amdgcn_mfma_f32_16x16x32_bf16(__builtin_bit_cast(s16x8, a), __builtin_bit_cast(s16x8, b), c, 0, 0, 0);
}

template <typename T> __device__ __forceinline__ T ldw(const void* p) {
  return __builtin_nontemporal_load(reinterpret_cast<const T*>(p));
}

__device__ __forceinline__ void put(u64* g, float v, unsigned tag) {
  __hip_atomic_store(g, ((u64)tag << 32) | (__float_as_uint(v) & 0xffffff00u), __ATOMIC_RELAXED, __HIP_MEMORY_SCOPE_AGENT);
}
__device__ __forceinline__ u64 get(const u64* g) {
  return __hip_atomic_load(g, __ATOMIC_RELAXED, __HIP_MEMORY_SCOPE_AGENT);
}
__device__ __forceinline__ float gval(u64 x) { return __uint_as_float((unsigned)x); }
__device__ __forceinline__ u32x4 get2(__amdgpu_buffer_rsrc_t r, unsigned off) {
  asm volatile("" ::: "memory");
  return __builtin_amdgcn_raw_buffer_load_b128(r, (int)off, 0, 16);
}
template <int N> __device__ __forceinline__ bool gall2(const u32x4 (&v)[N], unsigned tag) {
  bool ok = true; unsigned lo = 0;
#pragma unroll
  for (int k = 0; k < N; ++k) { ok &= (v[k][1] == tag) & (v[k][3] == tag); lo |= v[k][0] | v[k][2]; }
  ok &= (lo & 0xffu) == 0u;
  return __all(ok);
}
template <int N> __device__ __forceinline__ bool gall(const u64 (&v)[N], unsigned tag) {
  bool ok = true; unsigned lo = 0;
#pragma unroll
  for (int k = 0; k < N; ++k) { ok &= (unsigned)(v[k] >> 32) == tag; lo |= (unsigned)v[k]; }
  ok &= (lo & 0xffu) == 0u;
  return __all(ok);
}

__device__ __forceinline__ bool gok2(u32x4 v, unsigned tag) {
  return __all((v[1] == tag) & (v[3] == tag) & (((v[0] | v[2]) & 0xffu) == 0u));
}
__device__ __forceinline__ void bar() {
  asm volatile("s_waitcnt lgkmcnt(0)" ::: "memory");
  __builtin_amdgcn_s_barrier();
  asm volatile("" ::: "memory");
}
__device__ __forceinline__ void lds_fence() { asm volatile("s_waitcnt lgkmcnt(0)" ::: "memory"); }
#define PIN() do { asm volatile("" ::: "memory"); __builtin_amdgcn_sched_barrier(0); } while (0)

#define STAMP(k) do { } while (0)

struct Lds {
  float qpart[4][64];
  unsigned qs[4][64];
  float wmax[4][4];
  float wsum[4][4];
  unsigned zero[64];
  float opart[4][512];
  float wgt[8][32];
  unsigned short orow[8][32];
  float wpart[8][64];
  float hs[512];
  float hstat[8][2];
  unsigned short hn[8][64];
  float upart[8][32];
  unsigned short gs[8][32];
  float fpart[8][64];
  unsigned tag[4];
  unsigned qcnt[4];
  float pad[18 * 1024];
};

__global__ void __launch_bounds__(NTHR, 2)
fused_decode_kernel(const unsigned short* __restrict__ x, const unsigned short* __restrict__ kc,
                    const unsigned short* __restrict__ vc, const unsigned short* __restrict__ wq,
                    const unsigned short* __restrict__ wo, const unsigned short* __restrict__ w1,
                    const unsigned short* __restrict__ b1, const unsigned short* __restrict__ w2,
                    const unsigned short* __restrict__ b2, unsigned short* __restrict__ out,
                    unsigned char* ws) {
  __shared__ __attribute__((aligned(16))) Lds L;
  unsigned cp0;
  {
    u64 pc0;
    asm volatile("s_getpc_b64 %0" : "=s"(pc0));
    typedef const __attribute__((address_space(1))) unsigned* gptr;
    const unsigned o0 = threadIdx.x * 128u;
    cp0 = *(gptr)((pc0 & ~(u64)255) + (o0 < CODE_BYTES ? o0 : 0u));
  }
  asm volatile("" :: "s"(x), "s"(kc), "s"(vc), "s"(wq), "s"(wo), "s"(w1), "s"(b1), "s"(w2), "s"(b2), "s"(out), "s"(ws));
  const int tid = threadIdx.x, lane = tid & 63, l15 = lane & 15, lq = lane >> 4;
  const int wave = __builtin_amdgcn_readfirstlane(tid >> 6);
  const int b = blockIdx.x, g = b & 7, i = b >> 3;
  u64* const h1 = (u64*)(ws + WS_H1);
  u64* const h2o = (u64*)(ws + WS_H2O);
  u64* const h3 = (u64*)(ws + WS_H3);
  u64* const h4 = (u64*)(ws + WS_H4);
  const __amdgpu_buffer_rsrc_t wsr = __builtin_amdgcn_make_buffer_rsrc(ws, 0, (int)WS_END, 0x00020000);
  const f32x4 zero4 = {0.f, 0.f, 0.f, 0.f};
  const u32x4 zerou = {0u, 0u, 0u, 0u};
  unsigned tag;
  const int rb = 2 * g + ((i >> 3) & 1), cb = i & 7;
  const int mb_ = (i - 16) * 8 + g;
  unsigned w1v[16];
  u32x2 w2v[8];
  STAMP(0);

  if (wave < 4) {
    const int a = wave, kr = i >> 3, cq = i & 7;
    const u32x4 xv = *reinterpret_cast<const u32x4*>(x + lane * 8);
    const u64 ep = get(reinterpret_cast<const u64*>(ws + WS_EPOCH));
    u32x2 wqv[8];
#pragma unroll
    for (int j = 0; j < 8; ++j)
      wqv[j] = ldw<u32x2>(wq + (size_t)(kr * 128 + a * 32 + lq * 8 + j) * (HQ * DH) + g * 512 + cq * 64 + l15 * 4);
    if (tid == 0) L.qcnt[0] = 0u;
    if (a == 1) L.zero[lane] = 0u;
    PIN();
    bar();
    PIN();
    float xf[8];
#pragma unroll
    for (int k = 0; k < 4; ++k) { xf[2 * k] = bflo(xv[k]); xf[2 * k + 1] = bfhi(xv[k]); }
    float s1 = 0.f;
#pragma unroll
    for (int k = 0; k < 8; ++k) s1 += xf[k];
    const float mean = wave_sum(s1) * (1.f / DM);
    float s2 = 0.f;
#pragma unroll
    for (int k = 0; k < 8; ++k) { const float d = xf[k] - mean; s2 += d * d; }
    const float var = wave_sum(s2) * (1.f / DM);
    const float mb = bf16r(mean), rden = __builtin_amdgcn_rcpf(bf16r(__builtin_amdgcn_sqrtf(bf16r(bf16r(var) + 1e-5f))));
    u32x4 xn;
#pragma unroll
    for (int k = 0; k < 4; ++k) xn[k] = pk2(bf16r(xf[2 * k] - mb) * rden, bf16r(xf[2 * k + 1] - mb) * rden);
    u32x4 xa;
    {
      const int src = (kr * 16 + a * 4 + lq) * 4;
#pragma unroll
      for (int k = 0; k < 4; ++k) { const unsigned t = (unsigned)__builtin_amdgcn_ds_bpermute(src, (int)xn[k]); xa[k] = l15 == 0 ? t : 0u; }
    }
    tag = (unsigned)ep + 1u;
    if (tid == 0) L.tag[0] = tag;
    {
      f32x4 qc[4];
      u32x4 f0, f1, f2, f3;
#pragma unroll
      for (int k = 0; k < 4; ++k) {
        f0[k] = plo(wqv[2 * k][0], wqv[2 * k + 1][0]); f1[k] = phi(wqv[2 * k][0], wqv[2 * k + 1][0]);
        f2[k] = plo(wqv[2 * k][1], wqv[2 * k + 1][1]); f3[k] = phi(wqv[2 * k][1], wqv[2 * k + 1][1]);
      }
      qc[0] = mfma(xa, f0, zero4); qc[1] = mfma(xa, f1, zero4); qc[2] = mfma(xa, f2, zero4); qc[3] = mfma(xa, f3, zero4);
      if (lane < 16) *reinterpret_cast<f32x4*>(&L.qpart[a][lane * 4]) = f32x4{qc[0][0], qc[1][0], qc[2][0], qc[3][0]};
    }
    STAMP(1);
    lds_fence();
    if (a != 0) {
      if (lane == 0) __hip_atomic_fetch_add(&L.qcnt[0], 1u, __ATOMIC_RELAXED, __HIP_MEMORY_SCOPE_WORKGROUP);
    } else {
      for (unsigned spins = 0; spins < SPIN_CAP; ++spins) {
        if (__hip_atomic_load(&L.qcnt[0], __ATOMIC_RELAXED, __HIP_MEMORY_SCOPE_WORKGROUP) == 3u) break;
      }
      asm volatile("" ::: "memory");
      const float qv = (L.qpart[0][lane] + L.qpart[1][lane]) + (L.qpart[2][lane] + L.qpart[3][lane]);
      put(h1 + g * (H1STRIDE / 8) + kr * 512 + cq * 64 + lane, qv, tag);
    }
    PIN();
    bar();
    PIN();
    STAMP(2);
    {
      const unsigned src = (unsigned)WS_H1 + g * H1STRIDE + (a * 128 + lane * 2) * 8;
      u32x4 v[4];
      {
        const u64 t0 = __builtin_amdgcn_s_memrealtime();
        while (__builtin_amdgcn_s_memrealtime() - t0 < QDELAY) __builtin_amdgcn_s_sleep(1);
      }
      PIN();
#pragma unroll
      for (int k = 0; k < 4; ++k) v[k] = get2(wsr, src + k * 4096);
      PIN();
      bar();
      PIN();
      for (unsigned spins = 0; spins < SPIN_CAP; ++spins) {
        if (gall2(v, tag)) break;
#pragma unroll
        for (int k = 0; k < 4; ++k) v[k] = get2(wsr, src + k * 4096);
      }
      const float q0 = (__uint_as_float(v[0][0]) + __uint_as_float(v[1][0])) + (__uint_as_float(v[2][0]) + __uint_as_float(v[3][0]));
      const float q1 = (__uint_as_float(v[0][2]) + __uint_as_float(v[1][2])) + (__uint_as_float(v[2][2]) + __uint_as_float(v[3][2]));
      L.qs[a][lane] = pk2(q0, q1);
    }
    asm volatile("" :: "v"(cp0));
    STAMP(3);
    bar();
    PIN();
    PIN();
    bar();
    bar();
  } else {
    const int w4 = wave - 4;
    const size_t kvbase = ((size_t)g * SK + (SK - WIN) + i * 128 + w4 * 32) * DH;
    bar();
    PIN();
    u32x4 kf[2][4];
    u32x4 vv[8];
#pragma unroll
    for (int c = 0; c < 4; ++c) kf[0][c] = ldw<u32x4>(kc + kvbase + (size_t)l15 * DH + c * 32 + lq * 8);
    PIN();
    bar();
    PIN();
#pragma unroll
    for (int c = 0; c < 4; ++c) kf[1][c] = ldw<u32x4>(kc + kvbase + (size_t)(16 + l15) * DH + c * 32 + lq * 8);
#pragma unroll
    for (int j = 0; j < VEARLY; ++j) vv[j] = ldw<u32x4>(vc + kvbase + (size_t)((j >> 2) * 16 + lq * 4 + (j & 3)) * DH + l15 * 8);
    PIN();
    bar();
    PIN();
#pragma unroll
    for (int j = VEARLY; j < 8; ++j) vv[j] = ldw<u32x4>(vc + kvbase + (size_t)((j >> 2) * 16 + lq * 4 + (j & 3)) * DH + l15 * 8);
    PIN();
    tag = L.tag[0];
    bar();
    asm volatile("" :: "v"(cp0));
    STAMP(1);
    f32x4 s0 = zero4, s1 = zero4;
    {
      const unsigned* qsrc = l15 < 4 ? &L.qs[l15][lq * 4] : &L.zero[lq * 4];
      u32x4 qa[4];
#pragma unroll
      for (int c = 0; c < 4; ++c) qa[c] = *reinterpret_cast<const u32x4*>(qsrc + c * 16);
#pragma unroll
      for (int c = 0; c < 4; ++c) { s0 = mfma(kf[0][c], qa[c], s0); s1 = mfma(kf[1][c], qa[c], s1); }
    }
    float sc[8];
#pragma unroll
    for (int r = 0; r < 4; r += 2) {
      unsigned t = pk2(s0[r], s0[r + 1]);
      t = pk2(bflo(t) * 0.08838834764831845f, bfhi(t) * 0.08838834764831845f);
      sc[r] = bflo(t); sc[r + 1] = bfhi(t);
      t = pk2(s1[r], s1[r + 1]);
      t = pk2(bflo(t) * 0.08838834764831845f, bfhi(t) * 0.08838834764831845f);
      sc[4 + r] = bflo(t); sc[5 + r] = bfhi(t);
    }
    {
      float mx = fmaxf(fmaxf(fmaxf(sc[0], sc[1]), fmaxf(sc[2], sc[3])), fmaxf(fmaxf(sc[4], sc[5]), fmaxf(sc[6], sc[7])));
      mx = sw32_max(sw16_max(mx));
      if (lane < 4) L.wmax[lane][w4] = mx;
    }
    STAMP(2);
    bar();
    u32x4 pb;
    {
      const f32x4 m4 = *reinterpret_cast<const f32x4*>(&L.wmax[l15 & 3][0]);
      const float M = fmaxf(fmaxf(m4[0], m4[1]), fmaxf(m4[2], m4[3]));
      float pp[8];
#pragma unroll
      for (int r = 0; r < 8; ++r) pp[r] = __expf(sc[r] - M);
      float ls = ((pp[0] + pp[1]) + (pp[2] + pp[3])) + ((pp[4] + pp[5]) + (pp[6] + pp[7]));
      ls = sw32_sum(sw16_sum(ls));
      if (lane < 4) L.wsum[lane][w4] = ls;
#pragma unroll
      for (int r = 0; r < 4; ++r) pb[r] = pk2(pp[2 * r], pp[2 * r + 1]);
    }
    f32x4 oc[8];
#pragma unroll
    for (int c2 = 0; c2 < 4; ++c2) {
      u32x4 f0, f1;
#pragma unroll
      for (int k = 0; k < 4; ++k) { f0[k] = plo(vv[2 * k][c2], vv[2 * k + 1][c2]); f1[k] = phi(vv[2 * k][c2], vv[2 * k + 1][c2]); }
      oc[2 * c2] = mfma(f0, pb, zero4);
      oc[2 * c2 + 1] = mfma(f1, pb, zero4);
    }
    if (l15 < 4) {
#pragma unroll
      for (int r = 0; r < 4; ++r) {
        float* dst = &L.opart[w4][l15 * 128 + (lq * 4 + r) * 8];
        *reinterpret_cast<f32x4*>(dst) = f32x4{oc[0][r], oc[1][r], oc[2][r], oc[3][r]};
        *reinterpret_cast<f32x4*>(dst + 4) = f32x4{oc[4][r], oc[5][r], oc[6][r], oc[7][r]};
      }
    }
    STAMP(3);
    bar();
  }

  {
    const float o = (L.opart[0][tid] + L.opart[1][tid]) + (L.opart[2][tid] + L.opart[3][tid]);
    put(h2o + ((size_t)g * 32 + i) * H2B + tid, o, tag);
    if (tid < 4) {
      const float l = (L.wsum[tid][0] + L.wsum[tid][1]) + (L.wsum[tid][2] + L.wsum[tid][3]);
      const float m = fmaxf(fmaxf(L.wmax[tid][0], L.wmax[tid][1]), fmaxf(L.wmax[tid][2], L.wmax[tid][3]));
      put(h2o + ((size_t)g * 32 + i) * H2B + 512 + tid * 2, m, tag);
      put(h2o + ((size_t)g * 32 + i) * H2B + 513 + tid * 2, l, tag);
    }
  }
  STAMP(4);
  if (i < 16) {
    PIN();
    u32x2 wov[8];
#pragma unroll
    for (int j = 0; j < 8; ++j)
      wov[j] = ldw<u32x2>(wo + (size_t)(rb * 256 + wave * 32 + lq * 8 + j) * DM + cb * 64 + l15 * 4);
    PIN();
    const int hl = (rb & 1) * 2 + (wave >> 2);
    const unsigned so = (unsigned)WS_H2O + ((g * 32 + lq * 8) * H2B + hl * 128 + (wave & 3) * 32 + l15 * 2) * 8;
    const unsigned ss = (unsigned)WS_H2O + ((g * 32 + (lane & 31)) * H2B + 512 + hl * 2) * 8;
    u32x4 v[9];
    {
      const u64 t0 = __builtin_amdgcn_s_memrealtime();
      while (__builtin_amdgcn_s_memrealtime() - t0 < H2MODE) __builtin_amdgcn_s_sleep(1);
    }
    PIN();
    v[8] = get2(wsr, ss);
#pragma unroll
    for (int k = 0; k < 8; ++k) v[k] = get2(wsr, so + k * (H2B * 8));
    u32x4 f0, f1, f2, f3;
#pragma unroll
    for (int k = 0; k < 4; ++k) {
      f0[k] = plo(wov[2 * k][0], wov[2 * k + 1][0]); f1[k] = phi(wov[2 * k][0], wov[2 * k + 1][0]);
      f2[k] = plo(wov[2 * k][1], wov[2 * k + 1][1]); f3[k] = phi(wov[2 * k][1], wov[2 * k + 1][1]);
    }
    if (!gall2(v, tag)) {
      for (unsigned spins = 0; spins < SPIN_CAP; ++spins) {
        bool all = true;
        if (!gok2(v[8], tag)) { all = false; v[8] = get2(wsr, ss); }
#pragma unroll
        for (int k = 0; k < 8; ++k) { if (!gok2(v[k], tag)) { all = false; v[k] = get2(wsr, so + k * (H2B * 8)); } }
        if (all) break;
      }
    }
    STAMP(5);
    const float mm = __uint_as_float(v[8][0]);
    float M = sw16_max(row16_max(mm));
    const float wg = __expf(mm - M);
    if (lane < 32) L.wgt[wave][lane] = wg;
    const float Ls = sw16_sum(row16_sum(wg * __uint_as_float(v[8][2])));
    lds_fence();
    float o0 = 0.f, o1 = 0.f;
#pragma unroll
    for (int k4 = 0; k4 < 2; ++k4) {
      const f32x4 wv = *reinterpret_cast<const f32x4*>(&L.wgt[wave][lq * 8 + k4 * 4]);
#pragma unroll
      for (int k = 0; k < 4; ++k) { o0 = fmaf(wv[k], __uint_as_float(v[k4 * 4 + k][0]), o0); o1 = fmaf(wv[k], __uint_as_float(v[k4 * 4 + k][2]), o1); }
    }
    o0 = sw32_sum(sw16_sum(o0)); o1 = sw32_sum(sw16_sum(o1));
    const float rl = __builtin_amdgcn_rcpf(Ls);
    if (lane < 16) reinterpret_cast<unsigned*>(&L.orow[wave][0])[lane] = pk2(o0 * rl, o1 * rl);
    lds_fence();
    u32x4 oa = zerou;
    if (l15 == 0) oa = *reinterpret_cast<const u32x4*>(&L.orow[wave][lq * 8]);
    const f32x4 c0 = mfma(oa, f0, zero4), c1 = mfma(oa, f1, zero4), c2 = mfma(oa, f2, zero4), c3 = mfma(oa, f3, zero4);
    if (lane < 16) *reinterpret_cast<f32x4*>(&L.wpart[wave][lane * 4]) = f32x4{c0[0], c1[0], c2[0], c3[0]};
    bar();
    if (tid < 64) {
      float t = 0.f;
#pragma unroll
      for (int k = 0; k < 8; ++k) t += L.wpart[k][tid];
      put(h3 + (size_t)rb * 512 + cb * 64 + tid, t, tag);
    }
    STAMP(6);
    return;
  }
  if (mb_ >= NMLP + NFIN) return;
  if (mb_ >= NMLP) {
    const int fb = mb_ - NMLP;
    const unsigned xr = *reinterpret_cast<const unsigned*>(x + fb * 64 + (lane & 31) * 2);
    const unsigned short b2r = b2[fb * 64 + lane];
    if (wave == 0) {
      const unsigned src = (unsigned)WS_H3 + ((lane >> 5) * 8 * 512 + fb * 64 + (lane & 31) * 2) * 8;
      u32x4 v[8];
      for (unsigned spins = 0; spins < SPIN_CAP; ++spins) {
#pragma unroll
        for (int k = 0; k < 8; ++k) v[k] = get2(wsr, src + k * 4096);
        if (gall2(v, tag)) break;
        __builtin_amdgcn_s_sleep(1);
      }
      float t0 = 0.f, t1 = 0.f;
#pragma unroll
      for (int k = 0; k < 8; ++k) { t0 += __uint_as_float(v[k][0]); t1 += __uint_as_float(v[k][2]); }
      t0 = sw32_sum(t0); t1 = sw32_sum(t1);
      if (lane < 32) *reinterpret_cast<f32x2*>(&L.hs[lane * 2]) = f32x2{bf16r(bflo(xr) + bf16r(t0)), bf16r(bfhi(xr) + bf16r(t1))};
    }
    {
      u32x4 v[4];
      const int p0 = wave * 7 + (lane >> 5) * 4;
      const unsigned base = (unsigned)WS_H4 + (p0 * 512 + fb * 64 + (lane & 31) * 2) * 8;
      const unsigned last = (lane >> 5) ? 2u * 4096u : 3u * 4096u;
      for (unsigned spins = 0; spins < SPIN_CAP; ++spins) {
#pragma unroll
        for (int k = 0; k < 3; ++k) v[k] = get2(wsr, base + k * 4096);
        v[3] = get2(wsr, base + last);
        if (gall2(v, tag)) break;
        __builtin_amdgcn_s_sleep(1);
      }
      const float w3 = (lane >> 5) ? 0.f : 1.f;
      float t0 = (__uint_as_float(v[0][0]) + __uint_as_float(v[1][0])) + (__uint_as_float(v[2][0]) + w3 * __uint_as_float(v[3][0]));
      float t1 = (__uint_as_float(v[0][2]) + __uint_as_float(v[1][2])) + (__uint_as_float(v[2][2]) + w3 * __uint_as_float(v[3][2]));
      t0 = sw32_sum(t0); t1 = sw32_sum(t1);
      if (lane < 32) *reinterpret_cast<f32x2*>(&L.fpart[wave][lane * 2]) = f32x2{t0, t1};
    }
    STAMP(9);
    bar();
    if (tid < 64) {
      const int slot = (tid & 3) * 16 + (tid >> 2);
      float t = 0.f;
#pragma unroll
      for (int k = 0; k < 8; ++k) t += L.fpart[k][slot];
      const float y = bf16r(bf16r(t) + bflo(b2r));
      const float r = bf16r(L.hs[tid] + y);
      out[fb * 64 + tid] = (unsigned short)(__float_as_uint(r) >> 16);
      if (fb == 0 && tid == 0) *reinterpret_cast<u64*>(ws + WS_EPOCH) = (u64)tag;
    }
    STAMP(10);
    return;
  }

  PIN();
  {
#pragma unroll
    for (int j = 0; j < 16; ++j)
      w1v[j] = ldw<unsigned>(w1 + (size_t)(wave * 64 + (j >> 3) * 32 + lq * 8 + (j & 7)) * FF + mb_ * 32 + l15 * 2);
#pragma unroll
    for (int j = 0; j < 8; ++j)
      w2v[j] = ldw<u32x2>(w2 + (size_t)(mb_ * 32 + lq * 8 + j) * DM + wave * 64 + l15 * 4);
  }
  const unsigned xr = *reinterpret_cast<const unsigned*>(x + wave * 64 + (lane & 31) * 2);
  const unsigned short b1r = b1[mb_ * 32 + (lane & 31)];
  PIN();
  u32x4 e0[2], e1[2], f0, f1, f2, f3;
#pragma unroll
  for (int ks = 0; ks < 2; ++ks)
#pragma unroll
    for (int k = 0; k < 4; ++k) { e0[ks][k] = plo(w1v[ks * 8 + 2 * k], w1v[ks * 8 + 2 * k + 1]); e1[ks][k] = phi(w1v[ks * 8 + 2 * k], w1v[ks * 8 + 2 * k + 1]); }
#pragma unroll
  for (int k = 0; k < 4; ++k) {
    f0[k] = plo(w2v[2 * k][0], w2v[2 * k + 1][0]); f1[k] = phi(w2v[2 * k][0], w2v[2 * k + 1][0]);
    f2[k] = plo(w2v[2 * k][1], w2v[2 * k + 1][1]); f3[k] = phi(w2v[2 * k][1], w2v[2 * k + 1][1]);
  }
  PIN();
  float hv0, hv1;
  {
    const unsigned src = (unsigned)WS_H3 + ((lane >> 5) * 8 * 512 + wave * 64 + (lane & 31) * 2) * 8;
    u32x4 v[8];
    for (unsigned spins = 0; spins < SPIN_CAP; ++spins) {
#pragma unroll
      for (int k = 0; k < 8; ++k) v[k] = get2(wsr, src + k * 4096);
      if (gall2(v, tag)) break;
      __builtin_amdgcn_s_sleep(1);
    }
    float t0 = 0.f, t1 = 0.f;
#pragma unroll
    for (int k = 0; k < 8; ++k) { t0 += __uint_as_float(v[k][0]); t1 += __uint_as_float(v[k][2]); }
    t0 = sw32_sum(t0); t1 = sw32_sum(t1);
    hv0 = bf16r(bflo(xr) + bf16r(t0));
    hv1 = bf16r(bfhi(xr) + bf16r(t1));
    const float a1 = sw16_sum(row16_sum(hv0 + hv1)), a2 = sw16_sum(row16_sum(hv0 * hv0 + hv1 * hv1));
    if (lane == 0) *reinterpret_cast<f32x2*>(&L.hstat[wave][0]) = f32x2{a1, a2};
  }
  STAMP(7);
  bar();
  {
    float t1 = 0.f, t2 = 0.f;
#pragma unroll
    for (int k = 0; k < 4; ++k) { const f32x4 t = *reinterpret_cast<const f32x4*>(&L.hstat[2 * k][0]); t1 += t[0] + t[2]; t2 += t[1] + t[3]; }
    const float mean2 = t1 * (1.f / DM);
    const float var2 = fmaxf(t2 * (1.f / DM) - mean2 * mean2, 0.f);
    const float mb2 = bf16r(mean2), rden2 = __builtin_amdgcn_rcpf(bf16r(__builtin_amdgcn_sqrtf(bf16r(bf16r(var2) + 1e-5f))));
    if (lane < 32) reinterpret_cast<unsigned*>(&L.hn[wave][0])[lane] = pk2(bf16r(hv0 - mb2) * rden2, bf16r(hv1 - mb2) * rden2);
    lds_fence();
    f32x4 c0 = zero4, c1 = zero4;
#pragma unroll
    for (int ks = 0; ks < 2; ++ks) {
      u32x4 ha = zerou;
      if (l15 == 0) ha = *reinterpret_cast<const u32x4*>(&L.hn[wave][ks * 32 + lq * 8]);
      c0 = mfma(ha, e0[ks], c0); c1 = mfma(ha, e1[ks], c1);
    }
    if (lane < 16) *reinterpret_cast<f32x2*>(&L.upart[wave][lane * 2]) = f32x2{c0[0], c1[0]};
  }
  bar();
  {
    if (lane < 32) {
      float u = 0.f;
#pragma unroll
      for (int k = 0; k < 8; ++k) u += L.upart[k][lane];
      u = bf16r(bf16r(u) + bflo(b1r));
      const float z = 0.7978845608028654f * (u + 0.044715f * u * u * u);
      const float th = 1.f - 2.f * __builtin_amdgcn_rcpf(__expf(2.f * z) + 1.f);
      L.gs[wave][lane] = (unsigned short)(__float_as_uint(bf16r(0.5f * u * (1.f + th))) >> 16);
    }
    lds_fence();
    u32x4 ga = zerou;
    if (l15 == 0) ga = *reinterpret_cast<const u32x4*>(&L.gs[wave][lq * 8]);
    const f32x4 c0 = mfma(ga, f0, zero4), c1 = mfma(ga, f1, zero4), c2 = mfma(ga, f2, zero4), c3 = mfma(ga, f3, zero4);
    if (lane < 16) {
      u64* dst = h4 + (size_t)mb_ * 512 + wave * 64 + lane;
      put(dst, c0[0], tag); put(dst + 16, c1[0], tag); put(dst + 32, c2[0], tag); put(dst + 48, c3[0], tag);
    }
  }
  STAMP(8);
}

extern "C" void kernel_launch(void* const* d_in, const int* in_sizes, int n_in,
                              void* d_out, int out_size, void* d_ws, size_t ws_size,
                              hipStream_t stream) {
  const unsigned short* x = (const unsigned short*)d_in[0];
  const unsigned short* kc = (const unsigned short*)d_in[1];
  const unsigned short* vc = (const unsigned short*)d_in[2];
  const unsigned short* wq = (const unsigned short*)d_in[3];
  const unsigned short* wo = (const unsigned short*)d_in[4];
  const unsigned short* w1 = (const unsigned short*)d_in[6];
  const unsigned short* b1 = (const unsigned short*)d_in[7];
  const unsigned short* w2 = (const unsigned short*)d_in[8];
  const unsigned short* b2 = (const unsigned short*)d_in[9];
  fused_decode_kernel<<<NBLK, NTHR, 0, stream>>>(x, kc, vc, wq, wo, w1, b1, w2, b2,
                                                 (unsigned short*)d_out, (unsigned char*)d_ws);
}
```

```cpp
#define STAMPS 1
#include <cstdio>
#include <hip/hip_runtime.h>
#include <hip/hip_bf16.h>
#include <stdint.h>

typedef unsigned long long u64;
typedef __attribute__((ext_vector_type(4))) unsigned u32x4;
typedef __attribute__((ext_vector_type(2))) unsigned u32x2;
typedef __attribute__((ext_vector_type(4))) float f32x4;
typedef __attribute__((ext_vector_type(2))) float f32x2;
typedef __attribute__((ext_vector_type(8))) short s16x8;

constexpr int DM = 512, HQ = 32, HKV = 8, SK = 65536, DH = 128, WIN = 4096, FF = 1792;
constexpr int NBLK = 256, NTHR = 512;
constexpr int NMLP = 56, NFIN = 8;

constexpr size_t WS_EPOCH = 0;
#define H1STRIDE (128 * 1024)
constexpr size_t WS_H1 = 4096;
constexpr size_t WS_H2O = WS_H1 + (size_t)8 * H1STRIDE;
constexpr int H2B = 528;
constexpr size_t WS_H3 = WS_H2O + (size_t)8 * 32 * H2B * 8;
constexpr size_t WS_H4 = WS_H3 + (size_t)16 * 512 * 8;
constexpr size_t WS_END = WS_H4 + (size_t)NMLP * 512 * 8;
constexpr size_t WS_STAMP = (WS_END + 4095) / 4096 * 4096;

constexpr unsigned SPIN_CAP = 1u << 18;
#define QDELAY 100
#define H2MODE 110
#define CODEPF 1
constexpr unsigned CODE_BYTES = 12032;
#define VEARLY 3

#define KV_NT 1

__device__ __forceinline__ float bf16r(float x) { return __bfloat162float(__float2bfloat16(x)); }
__device__ __forceinline__ float bflo(unsigned u) { return __uint_as_float(u << 16); }
__device__ __forceinline__ float bfhi(unsigned u) { return __uint_as_float(u & 0xffff0000u); }
__device__ __forceinline__ unsigned pk2(float lo, float hi) {
  return (__float_as_uint(bf16r(lo)) >> 16) | (__float_as_uint(bf16r(hi)) & 0xffff0000u);
}
__device__ __forceinline__ unsigned plo(unsigned a, unsigned b) { return __builtin_amdgcn_perm(b, a, 0x05040100u); }
__device__ __forceinline__ unsigned phi(unsigned a, unsigned b) { return __builtin_amdgcn_perm(b, a, 0x07060302u); }

template <int CTRL> __device__ __forceinline__ float dpp(float x) {
  return __builtin_bit_cast(float, __builtin_amdgcn_mov_dpp(__builtin_bit_cast(int, x), CTRL, 0xf, 0xf, true));
}
constexpr int XOR1 = 0xB1, XOR2 = 0x4E, ROR4 = 0x124, ROR8 = 0x128;
__device__ __forceinline__ float sw16_sum(float x) {
  auto s = __builtin_amdgcn_permlane16_swap(__float_as_uint(x), __float_as_uint(x), false, false);
  return __uint_as_float(s[0]) + __uint_as_float(s[1]);
}
__device__ __forceinline__ float sw32_sum(float x) {
  auto s = __builtin_amdgcn_permlane32_swap(__float_as_uint(x), __float_as_uint(x), false, false);
  return __uint_as_float(s[0]) + __uint_as_float(s[1]);
}
__device__ __forceinline__ float sw16_max(float x) {
  auto s = __builtin_amdgcn_permlane16_swap(__float_as_uint(x), __float_as_uint(x), false, false);
  return fmaxf(__uint_as_float(s[0]), __uint_as_float(s[1]));
}
__device__ __forceinline__ float sw32_max(float x) {
  auto s = __builtin_amdgcn_permlane32_swap(__float_as_uint(x), __float_as_uint(x), false, false);
  return fmaxf(__uint_as_float(s[0]), __uint_as_float(s[1]));
}
__device__ __forceinline__ float row16_sum(float x) {
  x += dpp<XOR1>(x); x += dpp<XOR2>(x); x += dpp<ROR4>(x); x += dpp<ROR8>(x); return x;
}
__device__ __forceinline__ float row16_max(float x) {
  x = fmaxf(x, dpp<XOR1>(x)); x = fmaxf(x, dpp<XOR2>(x)); x = fmaxf(x, dpp<ROR4>(x)); x = fmaxf(x, dpp<ROR8>(x)); return x;
}
__device__ __forceinline__ float wave_sum(float x) { return sw32_sum(sw16_sum(row16_sum(x))); }
__device__ __forceinline__ f32x4 mfma(u32x4 a, u32x4 b, f32x4 c) {
  return __builtin_amdgcn_mfma_f32_16x16x32_bf16(__builtin_bit_cast(s16x8, a), __builtin_bit_cast(s16x8, b), c, 0, 0, 0);
}

template <typename T> __device__ __forceinline__ T ldw(const void* p) {
  return __builtin_nontemporal_load(reinterpret_cast<const T*>(p));
}

__device__ __forceinline__ void put(u64* g, float v, unsigned tag) {
  __hip_atomic_store(g, ((u64)tag << 32) | (__float_as_uint(v) & 0xffffff00u), __ATOMIC_RELAXED, __HIP_MEMORY_SCOPE_AGENT);
}
__device__ __forceinline__ u64 get(const u64* g) {
  return __hip_atomic_load(g, __ATOMIC_RELAXED, __HIP_MEMORY_SCOPE_AGENT);
}
__device__ __forceinline__ float gval(u64 x) { return __uint_as_float((unsigned)x); }
__device__ __forceinline__ u32x4 get2(__amdgpu_buffer_rsrc_t r, unsigned off) {
  asm volatile("" ::: "memory");
  return __builtin_amdgcn_raw_buffer_load_b128(r, (int)off, 0, 16);
}
template <int N> __device__ __forceinline__ bool gall2(const u32x4 (&v)[N], unsigned tag) {
  bool ok = true; unsigned lo = 0;
#pragma unroll
  for (int k = 0; k < N; ++k) { ok &= (v[k][1] == tag) & (v[k][3] == tag); lo |= v[k][0] | v[k][2]; }
  ok &= (lo & 0xffu) == 0u;
  return __all(ok);
}
template <int N> __device__ __forceinline__ bool gall(const u64 (&v)[N], unsigned tag) {
  bool ok = true; unsigned lo = 0;
#pragma unroll
  for (int k = 0; k < N; ++k) { ok &= (unsigned)(v[k] >> 32) == tag; lo |= (unsigned)v[k]; }
  ok &= (lo & 0xffu) == 0u;
  return __all(ok);
}

__device__ __forceinline__ bool gok2(u32x4 v, unsigned tag) {
  return __all((v[1] == tag) & (v[3] == tag) & (((v[0] | v[2]) & 0xffu) == 0u));
}
__device__ __forceinline__ void bar() {
  asm volatile("s_waitcnt lgkmcnt(0)" ::: "memory");
  __builtin_amdgcn_s_barrier();
  asm volatile("" ::: "memory");
}
__device__ __forceinline__ void lds_fence() { asm volatile("s_waitcnt lgkmcnt(0)" ::: "memory"); }
#define PIN() do { asm volatile("" ::: "memory"); __builtin_amdgcn_sched_barrier(0); } while (0)

#define STAMP(k) do { if (lane == 0) { __hip_atomic_store(((u64*)(ws + WS_STAMP)) + ((size_t)b * 8 + wave) * 16 + (k), (u64)__builtin_amdgcn_s_memrealtime(), __ATOMIC_RELAXED, __HIP_MEMORY_SCOPE_AGENT); } } while (0)

struct Lds {
  float qpart[4][64];
  unsigned qs[4][64];
  float wmax[4][4];
  float wsum[4][4];
  unsigned zero[64];
  float opart[4][512];
  float wgt[8][32];
  unsigned short orow[8][32];
  float wpart[8][64];
  float hs[512];
  float hstat[8][2];
  unsigned short hn[8][64];
  float upart[8][32];
  unsigned short gs[8][32];
  float fpart[8][64];
  unsigned tag[4];
  unsigned qcnt[4];
  float pad[18 * 1024];
};

__global__ void __launch_bounds__(NTHR, 2)
fused_decode_kernel(const unsigned short* __restrict__ x, const unsigned short* __restrict__ kc,
                    const unsigned short* __restrict__ vc, const unsigned short* __restrict__ wq,
                    const unsigned short* __restrict__ wo, const unsigned short* __restrict__ w1,
                    const unsigned short* __restrict__ b1, const unsigned short* __restrict__ w2,
                    const unsigned short* __restrict__ b2, unsigned short* __restrict__ out,
                    unsigned char* ws) {
  __shared__ __attribute__((aligned(16))) Lds L;
  unsigned cp0;
  {
    u64 pc0;
    asm volatile("s_getpc_b64 %0" : "=s"(pc0));
    typedef const __attribute__((address_space(1))) unsigned* gptr;
    const unsigned o0 = threadIdx.x * 128u;
    cp0 = *(gptr)((pc0 & ~(u64)255) + (o0 < CODE_BYTES ? o0 : 0u));
  }
  asm volatile("" :: "s"(x), "s"(kc), "s"(vc), "s"(wq), "s"(wo), "s"(w1), "s"(b1), "s"(w2), "s"(b2), "s"(out), "s"(ws));
  const int tid = threadIdx.x, lane = tid & 63, l15 = lane & 15, lq = lane >> 4;
  const int wave = __builtin_amdgcn_readfirstlane(tid >> 6);
  const int b = blockIdx.x, g = b & 7, i = b >> 3;
  u64* const h1 = (u64*)(ws + WS_H1);
  u64* const h2o = (u64*)(ws + WS_H2O);
  u64* const h3 = (u64*)(ws + WS_H3);
  u64* const h4 = (u64*)(ws + WS_H4);
  const __amdgpu_buffer_rsrc_t wsr = __builtin_amdgcn_make_buffer_rsrc(ws, 0, (int)WS_END, 0x00020000);
  const f32x4 zero4 = {0.f, 0.f, 0.f, 0.f};
  const u32x4 zerou = {0u, 0u, 0u, 0u};
  unsigned tag;
  const int rb = 2 * g + ((i >> 3) & 1), cb = i & 7;
  const int mb_ = (i - 16) * 8 + g;
  unsigned w1v[16];
  u32x2 w2v[8];
  STAMP(0);

  if (wave < 4) {
    const int a = wave, kr = i >> 3, cq = i & 7;
    const u32x4 xv = *reinterpret_cast<const u32x4*>(x + lane * 8);
    const u64 ep = get(reinterpret_cast<const u64*>(ws + WS_EPOCH));
    u32x2 wqv[8];
#pragma unroll
    for (int j = 0; j < 8; ++j)
      wqv[j] = ldw<u32x2>(wq + (size_t)(kr * 128 + a * 32 + lq * 8 + j) * (HQ * DH) + g * 512 + cq * 64 + l15 * 4);
    if (tid == 0) L.qcnt[0] = 0u;
    if (a == 1) L.zero[lane] = 0u;
    PIN();
    bar();
    PIN();
    float xf[8];
#pragma unroll
    for (int k = 0; k < 4; ++k) { xf[2 * k] = bflo(xv[k]); xf[2 * k + 1] = bfhi(xv[k]); }
    float s1 = 0.f;
#pragma unroll
    for (int k = 0; k < 8; ++k) s1 += xf[k];
    const float mean = wave_sum(s1) * (1.f / DM);
    float s2 = 0.f;
#pragma unroll
    for (int k = 0; k < 8; ++k) { const float d = xf[k] - mean; s2 += d * d; }
    const float var = wave_sum(s2) * (1.f / DM);
    const float mb = bf16r(mean), rden = __builtin_amdgcn_rcpf(bf16r(__builtin_amdgcn_sqrtf(bf16r(bf16r(var) + 1e-5f))));
    u32x4 xn;
#pragma unroll
    for (int k = 0; k < 4; ++k) xn[k] = pk2(bf16r(xf[2 * k] - mb) * rden, bf16r(xf[2 * k + 1] - mb) * rden);
    u32x4 xa;
    {
      const int src = (kr * 16 + a * 4 + lq) * 4;
#pragma unroll
      for (int k = 0; k < 4; ++k) { const unsigned t = (unsigned)__builtin_amdgcn_ds_bpermute(src, (int)xn[k]); xa[k] = l15 == 0 ? t : 0u; }
    }
    tag = (unsigned)ep + 1u;
    if (tid == 0) L.tag[0] = tag;
    {
      f32x4 qc[4];
      u32x4 f0, f1, f2, f3;
#pragma unroll
      for (int k = 0; k < 4; ++k) {
        f0[k] = plo(wqv[2 * k][0], wqv[2 * k + 1][0]); f1[k] = phi(wqv[2 * k][0], wqv[2 * k + 1][0]);
        f2[k] = plo(wqv[2 * k][1], wqv[2 * k + 1][1]); f3[k] = phi(wqv[2 * k][1], wqv[2 * k + 1][1]);
      }
      qc[0] = mfma(xa, f0, zero4); qc[1] = mfma(xa, f1, zero4); qc[2] = mfma(xa, f2, zero4); qc[3] = mfma(xa, f3, zero4);
      if (lane < 16) *reinterpret_cast<f32x4*>(&L.qpart[a][lane * 4]) = f32x4{qc[0][0], qc[1][0], qc[2][0], qc[3][0]};
    }
    STAMP(1);
    lds_fence();
    if (a != 0) {
      if (lane == 0) __hip_atomic_fetch_add(&L.qcnt[0], 1u, __ATOMIC_RELAXED, __HIP_MEMORY_SCOPE_WORKGROUP);
    } else {
      for (unsigned spins = 0; spins < SPIN_CAP; ++spins) {
        if (__hip_atomic_load(&L.qcnt[0], __ATOMIC_RELAXED, __HIP_MEMORY_SCOPE_WORKGROUP) == 3u) break;
      }
      asm volatile("" ::: "memory");
      const float qv = (L.qpart[0][lane] + L.qpart[1][lane]) + (L.qpart[2][lane] + L.qpart[3][lane]);
      put(h1 + g * (H1STRIDE / 8) + kr * 512 + cq * 64 + lane, qv, tag);
    }
    PIN();
    bar();
    PIN();
    STAMP(2);
    {
      const unsigned src = (unsigned)WS_H1 + g * H1STRIDE + (a * 128 + lane * 2) * 8;
      u32x4 v[4];
      {
        const u64 t0 = __builtin_amdgcn_s_memrealtime();
        while (__builtin_amdgcn_s_memrealtime() - t0 < QDELAY) __builtin_amdgcn_s_sleep(1);
      }
      PIN();
#pragma unroll
      for (int k = 0; k < 4; ++k) v[k] = get2(wsr, src + k * 4096);
      PIN();
      bar();
      PIN();
      for (unsigned spins = 0; spins < SPIN_CAP; ++spins) {
        if (gall2(v, tag)) break;
#pragma unroll
        for (int k = 0; k < 4; ++k) v[k] = get2(wsr, src + k * 4096);
      }
      const float q0 = (__uint_as_float(v[0][0]) + __uint_as_float(v[1][0])) + (__uint_as_float(v[2][0]) + __uint_as_float(v[3][0]));
      const float q1 = (__uint_as_float(v[0][2]) + __uint_as_float(v[1][2])) + (__uint_as_float(v[2][2]) + __uint_as_float(v[3][2]));
      L.qs[a][lane] = pk2(q0, q1);
    }
    asm volatile("" :: "v"(cp0));
    STAMP(3);
    bar();
    PIN();
    PIN();
    bar();
    bar();
  } else {
    const int w4 = wave - 4;
    const size_t kvbase = ((size_t)g * SK + (SK - WIN) + i * 128 + w4 * 32) * DH;
    bar();
    PIN();
    u32x4 kf[2][4];
    u32x4 vv[8];
#pragma unroll
    for (int c = 0; c < 4; ++c) kf[0][c] = ldw<u32x4>(kc + kvbase + (size_t)l15 * DH + c * 32 + lq * 8);
    PIN();
    bar();
    PIN();
#pragma unroll
    for (int c = 0; c < 4; ++c) kf[1][c] = ldw<u32x4>(kc + kvbase + (size_t)(16 + l15) * DH + c * 32 + lq * 8);
#pragma unroll
    for (int j = 0; j < VEARLY; ++j) vv[j] = ldw<u32x4>(vc + kvbase + (size_t)((j >> 2) * 16 + lq * 4 + (j & 3)) * DH + l15 * 8);
    PIN();
    bar();
    PIN();
#pragma unroll
    for (int j = VEARLY; j < 8; ++j) vv[j] = ldw<u32x4>(vc + kvbase + (size_t)((j >> 2) * 16 + lq * 4 + (j & 3)) * DH + l15 * 8);
    PIN();
    tag = L.tag[0];
    bar();
    asm volatile("" :: "v"(cp0));
    STAMP(1);
    f32x4 s0 = zero4, s1 = zero4;
    {
      const unsigned* qsrc = l15 < 4 ? &L.qs[l15][lq * 4] : &L.zero[lq * 4];
      u32x4 qa[4];
#pragma unroll
      for (int c = 0; c < 4; ++c) qa[c] = *reinterpret_cast<const u32x4*>(qsrc + c * 16);
#pragma unroll
      for (int c = 0; c < 4; ++c) { s0 = mfma(kf[0][c], qa[c], s0); s1 = mfma(kf[1][c], qa[c], s1); }
    }
    float sc[8];
#pragma unroll
    for (int r = 0; r < 4; r += 2) {
      unsigned t = pk2(s0[r], s0[r + 1]);
      t = pk2(bflo(t) * 0.08838834764831845f, bfhi(t) * 0.08838834764831845f);
      sc[r] = bflo(t); sc[r + 1] = bfhi(t);
      t = pk2(s1[r], s1[r + 1]);
      t = pk2(bflo(t) * 0.08838834764831845f, bfhi(t) * 0.08838834764831845f);
      sc[4 + r] = bflo(t); sc[5 + r] = bfhi(t);
    }
    {
      float mx = fmaxf(fmaxf(fmaxf(sc[0], sc[1]), fmaxf(sc[2], sc[3])), fmaxf(fmaxf(sc[4], sc[5]), fmaxf(sc[6], sc[7])));
      mx = sw32_max(sw16_max(mx));
      if (lane < 4) L.wmax[lane][w4] = mx;
    }
    STAMP(2);
    bar();
    u32x4 pb;
    {
      const f32x4 m4 = *reinterpret_cast<const f32x4*>(&L.wmax[l15 & 3][0]);
      const float M = fmaxf(fmaxf(m4[0], m4[1]), fmaxf(m4[2], m4[3]));
      float pp[8];
#pragma unroll
      for (int r = 0; r < 8; ++r) pp[r] = __expf(sc[r] - M);
      float ls = ((pp[0] + pp[1]) + (pp[2] + pp[3])) + ((pp[4] + pp[5]) + (pp[6] + pp[7]));
      ls = sw32_sum(sw16_sum(ls));
      if (lane < 4) L.wsum[lane][w4] = ls;
#pragma unroll
      for (int r = 0; r < 4; ++r) pb[r] = pk2(pp[2 * r], pp[2 * r + 1]);
    }
    f32x4 oc[8];
#pragma unroll
    for (int c2 = 0; c2 < 4; ++c2) {
      u32x4 f0, f1;
#pragma unroll
      for (int k = 0; k < 4; ++k) { f0[k] = plo(vv[2 * k][c2], vv[2 * k + 1][c2]); f1[k] = phi(vv[2 * k][c2], vv[2 * k + 1][c2]); }
      oc[2 * c2] = mfma(f0, pb, zero4);
      oc[2 * c2 + 1] = mfma(f1, pb, zero4);
    }
    if (l15 < 4) {
#pragma unroll
      for (int r = 0; r < 4; ++r) {
        float* dst = &L.opart[w4][l15 * 128 + (lq * 4 + r) * 8];
        *reinterpret_cast<f32x4*>(dst) = f32x4{oc[0][r], oc[1][r], oc[2][r], oc[3][r]};
        *reinterpret_cast<f32x4*>(dst + 4) = f32x4{oc[4][r], oc[5][r], oc[6][r], oc[7][r]};
      }
    }
    STAMP(3);
    bar();
  }

  {
    const float o = (L.opart[0][tid] + L.opart[1][tid]) + (L.opart[2][tid] + L.opart[3][tid]);
    put(h2o + ((size_t)g * 32 + i) * H2B + tid, o, tag);
    if (tid < 4) {
      const float l = (L.wsum[tid][0] + L.wsum[tid][1]) + (L.wsum[tid][2] + L.wsum[tid][3]);
      const float m = fmaxf(fmaxf(L.wmax[tid][0], L.wmax[tid][1]), fmaxf(L.wmax[tid][2], L.wmax[tid][3]));
      put(h2o + ((size_t)g * 32 + i) * H2B + 512 + tid * 2, m, tag);
      put(h2o + ((size_t)g * 32 + i) * H2B + 513 + tid * 2, l, tag);
    }
  }
  STAMP(4);
  if (i < 16) {
    PIN();
    u32x2 wov[8];
#pragma unroll
    for (int j = 0; j < 8; ++j)
      wov[j] = ldw<u32x2>(wo + (size_t)(rb * 256 + wave * 32 + lq * 8 + j) * DM + cb * 64 + l15 * 4);
    PIN();
    const int hl = (rb & 1) * 2 + (wave >> 2);
    const unsigned so = (unsigned)WS_H2O + ((g * 32 + lq * 8) * H2B + hl * 128 + (wave & 3) * 32 + l15 * 2) * 8;
    const unsigned ss = (unsigned)WS_H2O + ((g * 32 + (lane & 31)) * H2B + 512 + hl * 2) * 8;
    u32x4 v[9];
    {
      const u64 t0 = __builtin_amdgcn_s_memrealtime();
      while (__builtin_amdgcn_s_memrealtime() - t0 < H2MODE) __builtin_amdgcn_s_sleep(1);
    }
    PIN();
    v[8] = get2(wsr, ss);
#pragma unroll
    for (int k = 0; k < 8; ++k) v[k] = get2(wsr, so + k * (H2B * 8));
    u32x4 f0, f1, f2, f3;
#pragma unroll
    for (int k = 0; k < 4; ++k) {
      f0[k] = plo(wov[2 * k][0], wov[2 * k + 1][0]); f1[k] = phi(wov[2 * k][0], wov[2 * k + 1][0]);
      f2[k] = plo(wov[2 * k][1], wov[2 * k + 1][1]); f3[k] = phi(wov[2 * k][1], wov[2 * k + 1][1]);
    }
    if (!gall2(v, tag)) {
      for (unsigned spins = 0; spins < SPIN_CAP; ++spins) {
        bool all = true;
        if (!gok2(v[8], tag)) { all = false; v[8] = get2(wsr, ss); }
#pragma unroll
        for (int k = 0; k < 8; ++k) { if (!gok2(v[k], tag)) { all = false; v[k] = get2(wsr, so + k * (H2B * 8)); } }
        if (all) break;
      }
    }
    STAMP(5);
    const float mm = __uint_as_float(v[8][0]);
    float M = sw16_max(row16_max(mm));
    const float wg = __expf(mm - M);
    if (lane < 32) L.wgt[wave][lane] = wg;
    const float Ls = sw16_sum(row16_sum(wg * __uint_as_float(v[8][2])));
    lds_fence();
    float o0 = 0.f, o1 = 0.f;
#pragma unroll
    for (int k4 = 0; k4 < 2; ++k4) {
      const f32x4 wv = *reinterpret_cast<const f32x4*>(&L.wgt[wave][lq * 8 + k4 * 4]);
#pragma unroll
      for (int k = 0; k < 4; ++k) { o0 = fmaf(wv[k], __uint_as_float(v[k4 * 4 + k][0]), o0); o1 = fmaf(wv[k], __uint_as_float(v[k4 * 4 + k][2]), o1); }
    }
    o0 = sw32_sum(sw16_sum(o0)); o1 = sw32_sum(sw16_sum(o1));
    const float rl = __builtin_amdgcn_rcpf(Ls);
    if (lane < 16) reinterpret_cast<unsigned*>(&L.orow[wave][0])[lane] = pk2(o0 * rl, o1 * rl);
    lds_fence();
    u32x4 oa = zerou;
    if (l15 == 0) oa = *reinterpret_cast<const u32x4*>(&L.orow[wave][lq * 8]);
    const f32x4 c0 = mfma(oa, f0, zero4), c1 = mfma(oa, f1, zero4), c2 = mfma(oa, f2, zero4), c3 = mfma(oa, f3, zero4);
    if (lane < 16) *reinterpret_cast<f32x4*>(&L.wpart[wave][lane * 4]) = f32x4{c0[0], c1[0], c2[0], c3[0]};
    bar();
    if (tid < 64) {
      float t = 0.f;
#pragma unroll
      for (int k = 0; k < 8; ++k) t += L.wpart[k][tid];
      put(h3 + (size_t)rb * 512 + cb * 64 + tid, t, tag);
    }
    STAMP(6);
    return;
  }
  if (mb_ >= NMLP + NFIN) return;
  if (mb_ >= NMLP) {
    const int fb = mb_ - NMLP;
    const unsigned xr = *reinterpret_cast<const unsigned*>(x + fb * 64 + (lane & 31) * 2);
    const unsigned short b2r = b2[fb * 64 + lane];
    if (wave == 0) {
      const unsigned src = (unsigned)WS_H3 + ((lane >> 5) * 8 * 512 + fb * 64 + (lane & 31) * 2) * 8;
      u32x4 v[8];
      for (unsigned spins = 0; spins < SPIN_CAP; ++spins) {
#pragma unroll
        for (int k = 0; k < 8; ++k) v[k] = get2(wsr, src + k * 4096);
        if (gall2(v, tag)) break;
        __builtin_amdgcn_s_sleep(1);
      }
      float t0 = 0.f, t1 = 0.f;
#pragma unroll
      for (int k = 0; k < 8; ++k) { t0 += __uint_as_float(v[k][0]); t1 += __uint_as_float(v[k][2]); }
      t0 = sw32_sum(t0); t1 = sw32_sum(t1);
      if (lane < 32) *reinterpret_cast<f32x2*>(&L.hs[lane * 2]) = f32x2{bf16r(bflo(xr) + bf16r(t0)), bf16r(bfhi(xr) + bf16r(t1))};
    }
    {
      u32x4 v[4];
      const int p0 = wave * 7 + (lane >> 5) * 4;
      const unsigned base = (unsigned)WS_H4 + (p0 * 512 + fb * 64 + (lane & 31) * 2) * 8;
      const unsigned last = (lane >> 5) ? 2u * 4096u : 3u * 4096u;
      for (unsigned spins = 0; spins < SPIN_CAP; ++spins) {
#pragma unroll
        for (int k = 0; k < 3; ++k) v[k] = get2(wsr, base + k * 4096);
        v[3] = get2(wsr, base + last);
        if (gall2(v, tag)) break;
        __builtin_amdgcn_s_sleep(1);
      }
      const float w3 = (lane >> 5) ? 0.f : 1.f;
      float t0 = (__uint_as_float(v[0][0]) + __uint_as_float(v[1][0])) + (__uint_as_float(v[2][0]) + w3 * __uint_as_float(v[3][0]));
      float t1 = (__uint_as_float(v[0][2]) + __uint_as_float(v[1][2])) + (__uint_as_float(v[2][2]) + w3 * __uint_as_float(v[3][2]));
      t0 = sw32_sum(t0); t1 = sw32_sum(t1);
      if (lane < 32) *reinterpret_cast<f32x2*>(&L.fpart[wave][lane * 2]) = f32x2{t0, t1};
    }
    STAMP(9);
    bar();
    if (tid < 64) {
      const int slot = (tid & 3) * 16 + (tid >> 2);
      float t = 0.f;
#pragma unroll
      for (int k = 0; k < 8; ++k) t += L.fpart[k][slot];
      const float y = bf16r(bf16r(t) + bflo(b2r));
      const float r = bf16r(L.hs[tid] + y);
      out[fb * 64 + tid] = (unsigned short)(__float_as_uint(r) >> 16);
      if (fb == 0 && tid == 0) *reinterpret_cast<u64*>(ws + WS_EPOCH) = (u64)tag;
    }
    STAMP(10);
    if (fb == 0 && tid == 0) {
      const u64 t0w = __builtin_amdgcn_s_memrealtime();
      while (__builtin_amdgcn_s_memrealtime() - t0w < 3000) __builtin_amdgcn_s_sleep(8);
      const u64* st = (const u64*)(ws + WS_STAMP);
      u64 t0 = ~0ull;
      for (int bb = 0; bb < 256; ++bb) for (int w = 0; w < 8; ++w) { u64 xx = get(st + ((size_t)bb * 8 + w) * 16); t0 = xx < t0 ? xx : t0; }
      for (int k = 0; k <= 10; ++k) {
        for (int wsel = 0; wsel < 8; wsel += 4) {
          u64 mn = ~0ull, mxx = 0, sum = 0; int n = 0;
          for (int bb = 0; bb < 256; ++bb) { u64 xx = get(st + ((size_t)bb * 8 + wsel) * 16 + k); if (xx >= t0 && xx - t0 < 100000) { xx -= t0; mn = xx < mn ? xx : mn; mxx = xx > mxx ? xx : mxx; sum += xx; ++n; } }
          if (n) printf("stamp %d wave %d n %d min %d avg %d max %d\n", k, wsel, n, (int)mn, (int)(sum / n), (int)mxx);
        }
      }
    }
    return;
  }

  PIN();
  {
#pragma unroll
    for (int j = 0; j < 16; ++j)
      w1v[j] = ldw<unsigned>(w1 + (size_t)(wave * 64 + (j >> 3) * 32 + lq * 8 + (j & 7)) * FF + mb_ * 32 + l15 * 2);
#pragma unroll
    for (int j = 0; j < 8; ++j)
      w2v[j] = ldw<u32x2>(w2 + (size_t)(mb_ * 32 + lq * 8 + j) * DM + wave * 64 + l15 * 4);
  }
  const unsigned xr = *reinterpret_cast<const unsigned*>(x + wave * 64 + (lane & 31) * 2);
  const unsigned short b1r = b1[mb_ * 32 + (lane & 31)];
  PIN();
  u32x4 e0[2], e1[2], f0, f1, f2, f3;
#pragma unroll
  for (int ks = 0; ks < 2; ++ks)
#pragma unroll
    for (int k = 0; k < 4; ++k) { e0[ks][k] = plo(w1v[ks * 8 + 2 * k], w1v[ks * 8 + 2 * k + 1]); e1[ks][k] = phi(w1v[ks * 8 + 2 * k], w1v[ks * 8 + 2 * k + 1]); }
#pragma unroll
  for (int k = 0; k < 4; ++k) {
    f0[k] = plo(w2v[2 * k][0], w2v[2 * k + 1][0]); f1[k] = phi(w2v[2 * k][0], w2v[2 * k + 1][0]);
    f2[k] = plo(w2v[2 * k][1], w2v[2 * k + 1][1]); f3[k] = phi(w2v[2 * k][1], w2v[2 * k + 1][1]);
  }
  PIN();
  float hv0, hv1;
  {
    const unsigned src = (unsigned)WS_H3 + ((lane >> 5) * 8 * 512 + wave * 64 + (lane & 31) * 2) * 8;
    u32x4 v[8];
    for (unsigned spins = 0; spins < SPIN_CAP; ++spins) {
#pragma unroll
      for (int k = 0; k < 8; ++k) v[k] = get2(wsr, src + k * 4096);
      if (gall2(v, tag)) break;
      __builtin_amdgcn_s_sleep(1);
    }
    float t0 = 0.f, t1 = 0.f;
#pragma unroll
    for (int k = 0; k < 8; ++k) { t0 += __uint_as_float(v[k][0]); t1 += __uint_as_float(v[k][2]); }
    t0 = sw32_sum(t0); t1 = sw32_sum(t1);
    hv0 = bf16r(bflo(xr) + bf16r(t0));
    hv1 = bf16r(bfhi(xr) + bf16r(t1));
    const float a1 = sw16_sum(row16_sum(hv0 + hv1)), a2 = sw16_sum(row16_sum(hv0 * hv0 + hv1 * hv1));
    if (lane == 0) *reinterpret_cast<f32x2*>(&L.hstat[wave][0]) = f32x2{a1, a2};
  }
  STAMP(7);
  bar();
  {
    float t1 = 0.f, t2 = 0.f;
#pragma unroll
    for (int k = 0; k < 4; ++k) { const f32x4 t = *reinterpret_cast<const f32x4*>(&L.hstat[2 * k][0]); t1 += t[0] + t[2]; t2 += t[1] + t[3]; }
    const float mean2 = t1 * (1.f / DM);
    const float var2 = fmaxf(t2 * (1.f / DM) - mean2 * mean2, 0.f);
    const float mb2 = bf16r(mean2), rden2 = __builtin_amdgcn_rcpf(bf16r(__builtin_amdgcn_sqrtf(bf16r(bf16r(var2) + 1e-5f))));
    if (lane < 32) reinterpret_cast<unsigned*>(&L.hn[wave][0])[lane] = pk2(bf16r(hv0 - mb2) * rden2, bf16r(hv1 - mb2) * rden2);
    lds_fence();
    f32x4 c0 = zero4, c1 = zero4;
#pragma unroll
    for (int ks = 0; ks < 2; ++ks) {
      u32x4 ha = zerou;
      if (l15 == 0) ha = *reinterpret_cast<const u32x4*>(&L.hn[wave][ks * 32 + lq * 8]);
      c0 = mfma(ha, e0[ks], c0); c1 = mfma(ha, e1[ks], c1);
    }
    if (lane < 16) *reinterpret_cast<f32x2*>(&L.upart[wave][lane * 2]) = f32x2{c0[0], c1[0]};
  }
  bar();
  {
    if (lane < 32) {
      float u = 0.f;
#pragma unroll
      for (int k = 0; k < 8; ++k) u += L.upart[k][lane];
      u = bf16r(bf16r(u) + bflo(b1r));
      const float z = 0.7978845608028654f * (u + 0.044715f * u * u * u);
      const float th = 1.f - 2.f * __builtin_amdgcn_rcpf(__expf(2.f * z) + 1.f);
      L.gs[wave][lane] = (unsigned short)(__float_as_uint(bf16r(0.5f * u * (1.f + th))) >> 16);
    }
    lds_fence();
    u32x4 ga = zerou;
    if (l15 == 0) ga = *reinterpret_cast<const u32x4*>(&L.gs[wave][lq * 8]);
    const f32x4 c0 = mfma(ga, f0, zero4), c1 = mfma(ga, f1, zero4), c2 = mfma(ga, f2, zero4), c3 = mfma(ga, f3, zero4);
    if (lane < 16) {
      u64* dst = h4 + (size_t)mb_ * 512 + wave * 64 + lane;
      put(dst, c0[0], tag); put(dst + 16, c1[0], tag); put(dst + 32, c2[0], tag); put(dst + 48, c3[0], tag);
    }
  }
  STAMP(8);
}

extern "C" void kernel_launch(void* const* d_in, const int* in_sizes, int n_in,
                              void* d_out, int out_size, void* d_ws, size_t ws_size,
                              hipStream_t stream) {
  const unsigned short* x = (const unsigned short*)d_in[0];
  const unsigned short* kc = (const unsigned short*)d_in[1];
  const unsigned short* vc = (const unsigned short*)d_in[2];
  const unsigned short* wq = (const unsigned short*)d_in[3];
  const unsigned short* wo = (const unsigned short*)d_in[4];
  const unsigned short* w1 = (const unsigned short*)d_in[6];
  const unsigned short* b1 = (const unsigned short*)d_in[7];
  const unsigned short* w2 = (const unsigned short*)d_in[8];
  const unsigned short* b2 = (const unsigned short*)d_in[9];
  fused_decode_kernel<<<NBLK, NTHR, 0, stream>>>(x, kc, vc, wq, wo, w1, b1, w2, b2,
                                                 (unsigned short*)d_out, (unsigned char*)d_ws);
}
```

```cpp
#include <hip/hip_runtime.h>
#include <hip/hip_bf16.h>
#include <stdint.h>

typedef unsigned long long u64;
typedef __attribute__((ext_vector_type(4))) unsigned u32x4;
typedef __attribute__((ext_vector_type(2))) unsigned u32x2;
typedef __attribute__((ext_vector_type(4))) float f32x4;
typedef __attribute__((ext_vector_type(2))) float f32x2;
typedef __attribute__((ext_vector_type(8))) short s16x8;

constexpr int DM = 512, HQ = 32, HKV = 8, SK = 65536, DH = 128, WIN = 4096, FF = 1792;
constexpr int NBLK = 256, NTHR = 512;
constexpr int NMLP = 56, NFIN = 8;

constexpr size_t WS_EPOCH = 0;
#define H1STRIDE (128 * 1024)
constexpr size_t WS_H1 = 4096;
constexpr size_t WS_H2O = WS_H1 + (size_t)8 * H1STRIDE;
constexpr int H2B = 528;
constexpr size_t WS_H3 = WS_H2O + (size_t)8 * 32 * H2B * 8;
constexpr size_t WS_H4 = WS_H3 + (size_t)16 * 512 * 8;
constexpr size_t WS_END = WS_H4 + (size_t)NMLP * 512 * 8;

constexpr unsigned SPIN_CAP = 1u << 18;
#define QDELAY 100
#define H2MODE 110
#define CODEPF 1
constexpr unsigned CODE_BYTES = 12032;
#define VEARLY 3

#define KV_NT 1

__device__ __forceinline__ float bf16r(float x) { return __bfloat162float(__float2bfloat16(x)); }
__device__ __forceinline__ float bflo(unsigned u) { return __uint_as_float(u << 16); }
__device__ __forceinline__ float bfhi(unsigned u) { return __uint_as_float(u & 0xffff0000u); }
__device__ __forceinline__ unsigned pk2(float lo, float hi) {
  return (__float_as_uint(bf16r(lo)) >> 16) | (__float_as_uint(bf16r(hi)) & 0xffff0000u);
}
__device__ __forceinline__ unsigned plo(unsigned a, unsigned b) { return __builtin_amdgcn_perm(b, a, 0x05040100u); }
__device__ __forceinline__ unsigned phi(unsigned a, unsigned b) { return __builtin_amdgcn_perm(b, a, 0x07060302u); }

template <int CTRL> __device__ __forceinline__ float dpp(float x) {
  return __builtin_bit_cast(float, __builtin_amdgcn_mov_dpp(__builtin_bit_cast(int, x), CTRL, 0xf, 0xf, true));
}
constexpr int XOR1 = 0xB1, XOR2 = 0x4E, ROR4 = 0x124, ROR8 = 0x128;
__device__ __forceinline__ float sw16_sum(float x) {
  auto s = __builtin_amdgcn_permlane16_swap(__float_as_uint(x), __float_as_uint(x), false, false);
  return __uint_as_float(s[0]) + __uint_as_float(s[1]);
}
__device__ __forceinline__ float sw32_sum(float x) {
  auto s = __builtin_amdgcn_permlane32_swap(__float_as_uint(x), __float_as_uint(x), false, false);
  return __uint_as_float(s[0]) + __uint_as_float(s[1]);
}
__device__ __forceinline__ float sw16_max(float x) {
  auto s = __builtin_amdgcn_permlane16_swap(__float_as_uint(x), __float_as_uint(x), false, false);
  return fmaxf(__uint_as_float(s[0]), __uint_as_float(s[1]));
}
__device__ __forceinline__ float sw32_max(float x) {
  auto s = __builtin_amdgcn_permlane32_swap(__float_as_uint(x), __float_as_uint(x), false, false);
  return fmaxf(__uint_as_float(s[0]), __uint_as_float(s[1]));
}
__device__ __forceinline__ float row16_sum(float x) {
  x += dpp<XOR1>(x); x += dpp<XOR2>(x); x += dpp<ROR4>(x); x += dpp<ROR8>(x); return x;
}
__device__ __forceinline__ float row16_max(float x) {
  x = fmaxf(x, dpp<XOR1>(x)); x = fmaxf(x, dpp<XOR2>(x)); x = fmaxf(x, dpp<ROR4>(x)); x = fmaxf(x, dpp<ROR8>(x)); return x;
}
template <int CTRL> __device__ __forceinline__ unsigned dppu(unsigned x) {
  return (unsigned)__builtin_amdgcn_mov_dpp((int)x, CTRL, 0xf, 0xf, true);
}
__device__ __forceinline__ u32x4 quad_gather(unsigned x) {
  return u32x4{dppu<0x00>(x), dppu<0x55>(x), dppu<0xAA>(x), dppu<0xFF>(x)};
}
__device__ __forceinline__ float wave_sum(float x) { return sw32_sum(sw16_sum(row16_sum(x))); }
__device__ __forceinline__ f32x4 mfma(u32x4 a, u32x4 b, f32x4 c) {
  return __builtin_amdgcn_mfma_f32_16x16x32_bf16(__builtin_bit_cast(s16x8, a), __builtin_bit_cast(s16x8, b), c, 0, 0, 0);
}

template <typename T> __device__ __forceinline__ T ldw(const void* p) {
  return __builtin_nontemporal_load(reinterpret_cast<const T*>(p));
}

__device__ __forceinline__ void put(u64* g, float v, unsigned tag) {
  __hip_atomic_store(g, ((u64)tag << 32) | (__float_as_uint(v) & 0xffffff00u), __ATOMIC_RELAXED, __HIP_MEMORY_SCOPE_AGENT);
}
__device__ __forceinline__ u64 get(const u64* g) {
  return __hip_atomic_load(g, __ATOMIC_RELAXED, __HIP_MEMORY_SCOPE_AGENT);
}
__device__ __forceinline__ float gval(u64 x) { return __uint_as_float((unsigned)x); }
__device__ __forceinline__ u32x4 get2(__amdgpu_buffer_rsrc_t r, unsigned off) {
  asm volatile("" ::: "memory");
  return __builtin_amdgcn_raw_buffer_load_b128(r, (int)off, 0, 16);
}
template <int N> __device__ __forceinline__ bool gall2(const u32x4 (&v)[N], unsigned tag) {
  bool ok = true; unsigned lo = 0;
#pragma unroll
  for (int k = 0; k < N; ++k) { ok &= (v[k][1] == tag) & (v[k][3] == tag); lo |= v[k][0] | v[k][2]; }
  ok &= (lo & 0xffu) == 0u;
  return __all(ok);
}
template <int N> __device__ __forceinline__ bool gall(const u64 (&v)[N], unsigned tag) {
  bool ok = true; unsigned lo = 0;
#pragma unroll
  for (int k = 0; k < N; ++k) { ok &= (unsigned)(v[k] >> 32) == tag; lo |= (unsigned)v[k]; }
  ok &= (lo & 0xffu) == 0u;
  return __all(ok);
}

__device__ __forceinline__ bool gok2(u32x4 v, unsigned tag) {
  return __all((v[1] == tag) & (v[3] == tag) & (((v[0] | v[2]) & 0xffu) == 0u));
}
__device__ __forceinline__ void bar() {
  asm volatile("s_waitcnt lgkmcnt(0)" ::: "memory");
  __builtin_amdgcn_s_barrier();
  asm volatile("" ::: "memory");
}
__device__ __forceinline__ void lds_fence() { asm volatile("s_waitcnt lgkmcnt(0)" ::: "memory"); }
#define PIN() do { asm volatile("" ::: "memory"); __builtin_amdgcn_sched_barrier(0); } while (0)

#define STAMP(k) do { } while (0)

struct Lds {
  float qpart[4][64];
  unsigned qs[4][64];
  float wmax[4][4];
  float wsum[4][4];
  float opart[4][512];
  float wgt[8][32];
  unsigned short orow[8][32];
  float wpart[8][64];
  float hs[512];
  float hstat[8][2];
  unsigned short hn[8][64];
  float upart[8][32];
  unsigned short gs[8][32];
  float fpart[8][64];
  unsigned tag[4];
  unsigned qcnt[4];
  float pad[18 * 1024];
};

__global__ void __launch_bounds__(NTHR, 2)
fused_decode_kernel(const unsigned short* __restrict__ x, const unsigned short* __restrict__ kc,
                    const unsigned short* __restrict__ vc, const unsigned short* __restrict__ wq,
                    const unsigned short* __restrict__ wo, const unsigned short* __restrict__ w1,
                    const unsigned short* __restrict__ b1, const unsigned short* __restrict__ w2,
                    const unsigned short* __restrict__ b2, unsigned short* __restrict__ out,
                    unsigned char* ws) {
  __shared__ __attribute__((aligned(16))) Lds L;
  unsigned cp0;
  {
    u64 pc0;
    asm volatile("s_getpc_b64 %0" : "=s"(pc0));
    typedef const __attribute__((address_space(1))) unsigned* gptr;
    const unsigned o0 = threadIdx.x * 128u;
    cp0 = *(gptr)((pc0 & ~(u64)255) + (o0 < CODE_BYTES ? o0 : 0u));
  }
  asm volatile("" :: "s"(x), "s"(kc), "s"(vc), "s"(wq), "s"(wo), "s"(w1), "s"(b1), "s"(w2), "s"(b2), "s"(out), "s"(ws));
  const int tid = threadIdx.x, lane = tid & 63, l15 = lane & 15, lq = lane >> 4;
  const int wave = __builtin_amdgcn_readfirstlane(tid >> 6);
  const int b = blockIdx.x, g = b & 7, i = b >> 3;
  u64* const h1 = (u64*)(ws + WS_H1);
  u64* const h2o = (u64*)(ws + WS_H2O);
  u64* const h3 = (u64*)(ws + WS_H3);
  u64* const h4 = (u64*)(ws + WS_H4);
  const __amdgpu_buffer_rsrc_t wsr = __builtin_amdgcn_make_buffer_rsrc(ws, 0, (int)WS_END, 0x00020000);
  const f32x4 zero4 = {0.f, 0.f, 0.f, 0.f};
  const u32x4 zerou = {0u, 0u, 0u, 0u};
  unsigned tag;
  const int rb = 2 * g + ((i >> 3) & 1), cb = i & 7;
  const int mb_ = (i - 16) * 8 + g;
  unsigned w1v[16];
  u32x2 w2v[8];
  STAMP(0);

  if (wave < 4) {
    const int a = wave, kr = i >> 3, cq = i & 7;
    const u32x4 xv = *reinterpret_cast<const u32x4*>(x + lane * 8);
    const u64 ep = get(reinterpret_cast<const u64*>(ws + WS_EPOCH));
    u32x2 wqv[8];
#pragma unroll
    for (int j = 0; j < 8; ++j)
      wqv[j] = ldw<u32x2>(wq + (size_t)(kr * 128 + a * 32 + lq * 8 + j) * (HQ * DH) + g * 512 + cq * 64 + l15 * 4);
    if (tid == 0) L.qcnt[0] = 0u;
    PIN();
    bar();
    PIN();
    float xf[8];
#pragma unroll
    for (int k = 0; k < 4; ++k) { xf[2 * k] = bflo(xv[k]); xf[2 * k + 1] = bfhi(xv[k]); }
    float s1 = 0.f;
#pragma unroll
    for (int k = 0; k < 8; ++k) s1 += xf[k];
    const float mean = wave_sum(s1) * (1.f / DM);
    float s2 = 0.f;
#pragma unroll
    for (int k = 0; k < 8; ++k) { const float d = xf[k] - mean; s2 += d * d; }
    const float var = wave_sum(s2) * (1.f / DM);
    const float mb = bf16r(mean), rden = __builtin_amdgcn_rcpf(bf16r(__builtin_amdgcn_sqrtf(bf16r(bf16r(var) + 1e-5f))));
    u32x4 xn;
#pragma unroll
    for (int k = 0; k < 4; ++k) xn[k] = pk2(bf16r(xf[2 * k] - mb) * rden, bf16r(xf[2 * k + 1] - mb) * rden);
    u32x4 xa;
    {
      const int src = (kr * 16 + a * 4 + lq) * 4;
#pragma unroll
      for (int k = 0; k < 4; ++k) xa[k] = (unsigned)__builtin_amdgcn_ds_bpermute(src, (int)xn[k]);
    }
    tag = (unsigned)ep + 1u;
    if (tid == 0) L.tag[0] = tag;
    {
      f32x4 qc[4];
      u32x4 f0, f1, f2, f3;
#pragma unroll
      for (int k = 0; k < 4; ++k) {
        f0[k] = plo(wqv[2 * k][0], wqv[2 * k + 1][0]); f1[k] = phi(wqv[2 * k][0], wqv[2 * k + 1][0]);
        f2[k] = plo(wqv[2 * k][1], wqv[2 * k + 1][1]); f3[k] = phi(wqv[2 * k][1], wqv[2 * k + 1][1]);
      }
      qc[0] = mfma(xa, f0, zero4); qc[1] = mfma(xa, f1, zero4); qc[2] = mfma(xa, f2, zero4); qc[3] = mfma(xa, f3, zero4);
      if (lane < 16) *reinterpret_cast<f32x4*>(&L.qpart[a][lane * 4]) = f32x4{qc[0][0], qc[1][0], qc[2][0], qc[3][0]};
    }
    STAMP(1);
    lds_fence();
    if (a != 0) {
      if (lane == 0) __hip_atomic_fetch_add(&L.qcnt[0], 1u, __ATOMIC_RELAXED, __HIP_MEMORY_SCOPE_WORKGROUP);
    } else {
      for (unsigned spins = 0; spins < SPIN_CAP; ++spins) {
        if (__hip_atomic_load(&L.qcnt[0], __ATOMIC_RELAXED, __HIP_MEMORY_SCOPE_WORKGROUP) == 3u) break;
      }
      asm volatile("" ::: "memory");
      const float qv = (L.qpart[0][lane] + L.qpart[1][lane]) + (L.qpart[2][lane] + L.qpart[3][lane]);
      put(h1 + g * (H1STRIDE / 8) + kr * 512 + cq * 64 + lane, qv, tag);
    }
    PIN();
    bar();
    PIN();
    STAMP(2);
    {
      const unsigned src = (unsigned)WS_H1 + g * H1STRIDE + (a * 128 + lane * 2) * 8;
      u32x4 v[4];
      {
        const u64 t0 = __builtin_amdgcn_s_memrealtime();
        while (__builtin_amdgcn_s_memrealtime() - t0 < QDELAY) __builtin_amdgcn_s_sleep(1);
      }
      PIN();
#pragma unroll
      for (int k = 0; k < 4; ++k) v[k] = get2(wsr, src + k * 4096);
      PIN();
      bar();
      PIN();
      for (unsigned spins = 0; spins < SPIN_CAP; ++spins) {
        if (gall2(v, tag)) break;
#pragma unroll
        for (int k = 0; k < 4; ++k) v[k] = get2(wsr, src + k * 4096);
      }
      const float q0 = (__uint_as_float(v[0][0]) + __uint_as_float(v[1][0])) + (__uint_as_float(v[2][0]) + __uint_as_float(v[3][0]));
      const float q1 = (__uint_as_float(v[0][2]) + __uint_as_float(v[1][2])) + (__uint_as_float(v[2][2]) + __uint_as_float(v[3][2]));
      L.qs[a][lane] = pk2(q0, q1);
    }
    asm volatile("" :: "v"(cp0));
    STAMP(3);
    bar();
    PIN();
    PIN();
    bar();
    bar();
  } else {
    const int w4 = wave - 4;
    const size_t kvbase = ((size_t)g * SK + (SK - WIN) + i * 128 + w4 * 32) * DH;
    bar();
    PIN();
    u32x4 kf[2][4];
    u32x4 vv[8];
#pragma unroll
    for (int c = 0; c < 4; ++c) kf[0][c] = ldw<u32x4>(kc + kvbase + (size_t)l15 * DH + c * 32 + lq * 8);
    PIN();
    bar();
    PIN();
#pragma unroll
    for (int c = 0; c < 4; ++c) kf[1][c] = ldw<u32x4>(kc + kvbase + (size_t)(16 + l15) * DH + c * 32 + lq * 8);
#pragma unroll
    for (int j = 0; j < VEARLY; ++j) vv[j] = ldw<u32x4>(vc + kvbase + (size_t)((j >> 2) * 16 + lq * 4 + (j & 3)) * DH + l15 * 8);
    PIN();
    bar();
    PIN();
#pragma unroll
    for (int j = VEARLY; j < 8; ++j) vv[j] = ldw<u32x4>(vc + kvbase + (size_t)((j >> 2) * 16 + lq * 4 + (j & 3)) * DH + l15 * 8);
    PIN();
    tag = L.tag[0];
    bar();
    asm volatile("" :: "v"(cp0));
    STAMP(1);
    f32x4 s0 = zero4, s1 = zero4;
    {
      const unsigned* qsrc = &L.qs[l15 & 3][lq * 4];
      u32x4 qa[4];
#pragma unroll
      for (int c = 0; c < 4; ++c) qa[c] = *reinterpret_cast<const u32x4*>(qsrc + c * 16);
#pragma unroll
      for (int c = 0; c < 4; ++c) { s0 = mfma(kf[0][c], qa[c], s0); s1 = mfma(kf[1][c], qa[c], s1); }
    }
    float sc[8];
#pragma unroll
    for (int r = 0; r < 4; r += 2) {
      unsigned t = pk2(s0[r], s0[r + 1]);
      t = pk2(bflo(t) * 0.08838834764831845f, bfhi(t) * 0.08838834764831845f);
      sc[r] = bflo(t); sc[r + 1] = bfhi(t);
      t = pk2(s1[r], s1[r + 1]);
      t = pk2(bflo(t) * 0.08838834764831845f, bfhi(t) * 0.08838834764831845f);
      sc[4 + r] = bflo(t); sc[5 + r] = bfhi(t);
    }
    {
      float mx = fmaxf(fmaxf(fmaxf(sc[0], sc[1]), fmaxf(sc[2], sc[3])), fmaxf(fmaxf(sc[4], sc[5]), fmaxf(sc[6], sc[7])));
      mx = sw32_max(sw16_max(mx));
      if (lane < 4) L.wmax[lane][w4] = mx;
    }
    STAMP(2);
    bar();
    u32x4 pb;
    {
      const f32x4 m4 = *reinterpret_cast<const f32x4*>(&L.wmax[l15 & 3][0]);
      const float M = fmaxf(fmaxf(m4[0], m4[1]), fmaxf(m4[2], m4[3]));
      float pp[8];
#pragma unroll
      for (int r = 0; r < 8; ++r) pp[r] = __expf(sc[r] - M);
      float ls = ((pp[0] + pp[1]) + (pp[2] + pp[3])) + ((pp[4] + pp[5]) + (pp[6] + pp[7]));
      ls = sw32_sum(sw16_sum(ls));
      if (lane < 4) L.wsum[lane][w4] = ls;
#pragma unroll
      for (int r = 0; r < 4; ++r) pb[r] = pk2(pp[2 * r], pp[2 * r + 1]);
    }
    f32x4 oc[8];
#pragma unroll
    for (int c2 = 0; c2 < 4; ++c2) {
      u32x4 f0, f1;
#pragma unroll
      for (int k = 0; k < 4; ++k) { f0[k] = plo(vv[2 * k][c2], vv[2 * k + 1][c2]); f1[k] = phi(vv[2 * k][c2], vv[2 * k + 1][c2]); }
      oc[2 * c2] = mfma(f0, pb, zero4);
      oc[2 * c2 + 1] = mfma(f1, pb, zero4);
    }
    if (l15 < 4) {
#pragma unroll
      for (int r = 0; r < 4; ++r) {
        float* dst = &L.opart[w4][l15 * 128 + (lq * 4 + r) * 8];
        *reinterpret_cast<f32x4*>(dst) = f32x4{oc[0][r], oc[1][r], oc[2][r], oc[3][r]};
        *reinterpret_cast<f32x4*>(dst + 4) = f32x4{oc[4][r], oc[5][r], oc[6][r], oc[7][r]};
      }
    }
    STAMP(3);
    bar();
  }

  {
    const float o = (L.opart[0][tid] + L.opart[1][tid]) + (L.opart[2][tid] + L.opart[3][tid]);
    put(h2o + ((size_t)g * 32 + i) * H2B + tid, o, tag);
    if (tid < 4) {
      const float l = (L.wsum[tid][0] + L.wsum[tid][1]) + (L.wsum[tid][2] + L.wsum[tid][3]);
      const float m = fmaxf(fmaxf(L.wmax[tid][0], L.wmax[tid][1]), fmaxf(L.wmax[tid][2], L.wmax[tid][3]));
      put(h2o + ((size_t)g * 32 + i) * H2B + 512 + tid * 2, m, tag);
      put(h2o + ((size_t)g * 32 + i) * H2B + 513 + tid * 2, l, tag);
    }
  }
  STAMP(4);
  if (i < 16) {
    PIN();
    u32x2 wov[8];
#pragma unroll
    for (int j = 0; j < 8; ++j)
      wov[j] = ldw<u32x2>(wo + (size_t)(rb * 256 + wave * 32 + lq * 8 + j) * DM + cb * 64 + l15 * 4);
    PIN();
    const int hl = (rb & 1) * 2 + (wave >> 2);
    const unsigned so = (unsigned)WS_H2O + ((g * 32 + (l15 >> 2) * 8) * H2B + hl * 128 + (wave & 3) * 32 + lq * 8 + (l15 & 3) * 2) * 8;
    const unsigned ss = (unsigned)WS_H2O + ((g * 32 + (lane & 31)) * H2B + 512 + hl * 2) * 8;
    u32x4 v[9];
    {
      const u64 t0 = __builtin_amdgcn_s_memrealtime();
      while (__builtin_amdgcn_s_memrealtime() - t0 < H2MODE) __builtin_amdgcn_s_sleep(1);
    }
    PIN();
    v[8] = get2(wsr, ss);
#pragma unroll
    for (int k = 0; k < 8; ++k) v[k] = get2(wsr, so + k * (H2B * 8));
    PIN();
    u32x4 f0, f1, f2, f3;
#pragma unroll
    for (int k = 0; k < 4; ++k) {
      f0[k] = plo(wov[2 * k][0], wov[2 * k + 1][0]); f1[k] = phi(wov[2 * k][0], wov[2 * k + 1][0]);
      f2[k] = plo(wov[2 * k][1], wov[2 * k + 1][1]); f3[k] = phi(wov[2 * k][1], wov[2 * k + 1][1]);
    }
    asm volatile("" : "+v"(f0), "+v"(f1), "+v"(f2), "+v"(f3));
    if (!gall2(v, tag)) {
      for (unsigned spins = 0; spins < SPIN_CAP; ++spins) {
        bool all = true;
        if (!gok2(v[8], tag)) { all = false; v[8] = get2(wsr, ss); }
#pragma unroll
        for (int k = 0; k < 8; ++k) { if (!gok2(v[k], tag)) { all = false; v[k] = get2(wsr, so + k * (H2B * 8)); } }
        if (all) break;
      }
    }
    STAMP(5);
    const float mm = __uint_as_float(v[8][0]);
    float M = sw16_max(row16_max(mm));
    const float wg = __expf(mm - M);
    if (lane < 32) L.wgt[wave][lane] = wg;
    const float Ls = sw16_sum(row16_sum(wg * __uint_as_float(v[8][2])));
    lds_fence();
    float o0 = 0.f, o1 = 0.f;
#pragma unroll
    for (int k4 = 0; k4 < 2; ++k4) {
      const f32x4 wv = *reinterpret_cast<const f32x4*>(&L.wgt[wave][(l15 >> 2) * 8 + k4 * 4]);
#pragma unroll
      for (int k = 0; k < 4; ++k) { o0 = fmaf(wv[k], __uint_as_float(v[k4 * 4 + k][0]), o0); o1 = fmaf(wv[k], __uint_as_float(v[k4 * 4 + k][2]), o1); }
    }
    o0 += dpp<ROR4>(o0); o0 += dpp<ROR8>(o0); o1 += dpp<ROR4>(o1); o1 += dpp<ROR8>(o1);
    const float rl = __builtin_amdgcn_rcpf(Ls);
    const u32x4 oa = quad_gather(pk2(o0 * rl, o1 * rl));
    const f32x4 c0 = mfma(oa, f0, zero4), c1 = mfma(oa, f1, zero4), c2 = mfma(oa, f2, zero4), c3 = mfma(oa, f3, zero4);
    if (lane < 16) *reinterpret_cast<f32x4*>(&L.wpart[wave][lane * 4]) = f32x4{c0[0], c1[0], c2[0], c3[0]};
    bar();
    if (tid < 64) {
      float t = 0.f;
#pragma unroll
      for (int k = 0; k < 8; ++k) t += L.wpart[k][tid];
      put(h3 + (size_t)rb * 512 + cb * 64 + tid, t, tag);
    }
    STAMP(6);
    return;
  }
  if (mb_ >= NMLP + NFIN) return;
  if (mb_ >= NMLP) {
    const int fb = mb_ - NMLP;
    const unsigned xr = *reinterpret_cast<const unsigned*>(x + fb * 64 + (lane & 31) * 2);
    const unsigned short b2r = b2[fb * 64 + lane];
    if (wave == 0) {
      const unsigned src = (unsigned)WS_H3 + ((lane >> 5) * 8 * 512 + fb * 64 + (lane & 31) * 2) * 8;
      u32x4 v[8];
      for (unsigned spins = 0; spins < SPIN_CAP; ++spins) {
#pragma unroll
        for (int k = 0; k < 8; ++k) v[k] = get2(wsr, src + k * 4096);
        if (gall2(v, tag)) break;
        __builtin_amdgcn_s_sleep(1);
      }
      float t0 = 0.f, t1 = 0.f;
#pragma unroll
      for (int k = 0; k < 8; ++k) { t0 += __uint_as_float(v[k][0]); t1 += __uint_as_float(v[k][2]); }
      t0 = sw32_sum(t0); t1 = sw32_sum(t1);
      if (lane < 32) *reinterpret_cast<f32x2*>(&L.hs[lane * 2]) = f32x2{bf16r(bflo(xr) + bf16r(t0)), bf16r(bfhi(xr) + bf16r(t1))};
    }
    {
      u32x4 v[4];
      const int p0 = wave * 7 + (lane >> 5) * 4;
      const unsigned base = (unsigned)WS_H4 + (p0 * 512 + fb * 64 + (lane & 31) * 2) * 8;
      const unsigned last = (lane >> 5) ? 2u * 4096u : 3u * 4096u;
      for (unsigned spins = 0; spins < SPIN_CAP; ++spins) {
#pragma unroll
        for (int k = 0; k < 3; ++k) v[k] = get2(wsr, base + k * 4096);
        v[3] = get2(wsr, base + last);
        if (gall2(v, tag)) break;
        __builtin_amdgcn_s_sleep(1);
      }
      const float w3 = (lane >> 5) ? 0.f : 1.f;
      float t0 = (__uint_as_float(v[0][0]) + __uint_as_float(v[1][0])) + (__uint_as_float(v[2][0]) + w3 * __uint_as_float(v[3][0]));
      float t1 = (__uint_as_float(v[0][2]) + __uint_as_float(v[1][2])) + (__uint_as_float(v[2][2]) + w3 * __uint_as_float(v[3][2]));
      t0 = sw32_sum(t0); t1 = sw32_sum(t1);
      if (lane < 32) *reinterpret_cast<f32x2*>(&L.fpart[wave][lane * 2]) = f32x2{t0, t1};
    }
    STAMP(9);
    bar();
    if (tid < 64) {
      const int slot = (tid & 3) * 16 + (tid >> 2);
      float t = 0.f;
#pragma unroll
      for (int k = 0; k < 8; ++k) t += L.fpart[k][slot];
      const float y = bf16r(bf16r(t) + bflo(b2r));
      const float r = bf16r(L.hs[tid] + y);
      out[fb * 64 + tid] = (unsigned short)(__float_as_uint(r) >> 16);
      if (fb == 0 && tid == 0) *reinterpret_cast<u64*>(ws + WS_EPOCH) = (u64)tag;
    }
    STAMP(10);
    return;
  }

  PIN();
  {
#pragma unroll
    for (int j = 0; j < 16; ++j)
      w1v[j] = ldw<unsigned>(w1 + (size_t)(wave * 64 + (j >> 3) * 32 + lq * 8 + (j & 7)) * FF + mb_ * 32 + l15 * 2);
#pragma unroll
    for (int j = 0; j < 8; ++j)
      w2v[j] = ldw<u32x2>(w2 + (size_t)(mb_ * 32 + lq * 8 + j) * DM + wave * 64 + l15 * 4);
  }
  const int cp = ((l15 >> 2) & 1) * 16 + lq * 4 + (l15 & 3);
  const unsigned xr = *reinterpret_cast<const unsigned*>(x + wave * 64 + cp * 2);
  const unsigned b1r = *reinterpret_cast<const unsigned*>(b1 + mb_ * 32 + lq * 8 + (l15 & 3) * 2);
  PIN();
  u32x4 e0[2], e1[2], f0, f1, f2, f3;
#pragma unroll
  for (int ks = 0; ks < 2; ++ks)
#pragma unroll
    for (int k = 0; k < 4; ++k) { e0[ks][k] = plo(w1v[ks * 8 + 2 * k], w1v[ks * 8 + 2 * k + 1]); e1[ks][k] = phi(w1v[ks * 8 + 2 * k], w1v[ks * 8 + 2 * k + 1]); }
#pragma unroll
  for (int k = 0; k < 4; ++k) {
    f0[k] = plo(w2v[2 * k][0], w2v[2 * k + 1][0]); f1[k] = phi(w2v[2 * k][0], w2v[2 * k + 1][0]);
    f2[k] = plo(w2v[2 * k][1], w2v[2 * k + 1][1]); f3[k] = phi(w2v[2 * k][1], w2v[2 * k + 1][1]);
  }
  PIN();
  asm volatile("" : "+v"(e0[0]), "+v"(e0[1]), "+v"(e1[0]), "+v"(e1[1]), "+v"(f0), "+v"(f1), "+v"(f2), "+v"(f3));
  float hv0, hv1;
  {
    const unsigned src = (unsigned)WS_H3 + ((l15 >> 3) * 8 * 512 + wave * 64 + cp * 2) * 8;
    u32x4 v[8];
    for (unsigned spins = 0; spins < SPIN_CAP; ++spins) {
#pragma unroll
      for (int k = 0; k < 8; ++k) v[k] = get2(wsr, src + k * 4096);
      if (gall2(v, tag)) break;
      __builtin_amdgcn_s_sleep(1);
    }
    float t0 = 0.f, t1 = 0.f;
#pragma unroll
    for (int k = 0; k < 8; ++k) { t0 += __uint_as_float(v[k][0]); t1 += __uint_as_float(v[k][2]); }
    t0 += dpp<ROR8>(t0); t1 += dpp<ROR8>(t1);
    hv0 = bf16r(bflo(xr) + bf16r(t0));
    hv1 = bf16r(bfhi(xr) + bf16r(t1));
    const float a1 = wave_sum(hv0 + hv1), a2 = wave_sum(hv0 * hv0 + hv1 * hv1);
    if (lane == 0) *reinterpret_cast<f32x2*>(&L.hstat[wave][0]) = f32x2{a1, a2};
  }
  STAMP(7);
  bar();
  {
    float t1 = 0.f, t2 = 0.f;
#pragma unroll
    for (int k = 0; k < 4; ++k) { const f32x4 t = *reinterpret_cast<const f32x4*>(&L.hstat[2 * k][0]); t1 += t[0] + t[2]; t2 += t[1] + t[3]; }
    const float mean2 = t1 * (0.5f / DM);
    const float var2 = fmaxf(t2 * (0.5f / DM) - mean2 * mean2, 0.f);
    const float mb2 = bf16r(mean2), rden2 = __builtin_amdgcn_rcpf(bf16r(__builtin_amdgcn_sqrtf(bf16r(bf16r(var2) + 1e-5f))));
    const u32x4 ha = quad_gather(pk2(bf16r(hv0 - mb2) * rden2, bf16r(hv1 - mb2) * rden2));
    const f32x4 c0a = mfma(ha, e0[0], zero4), c1a = mfma(ha, e1[0], zero4), c0b = mfma(ha, e0[1], zero4), c1b = mfma(ha, e1[1], zero4);
    const float u0 = sw16_sum(lane < 16 ? c0a[0] : c0b[0]), u1 = sw16_sum(lane < 16 ? c1a[0] : c1b[0]);
    if (lane < 16) *reinterpret_cast<f32x2*>(&L.upart[wave][lane * 2]) = f32x2{u0, u1};
  }
  bar();
  {
    float u0 = 0.f, u1 = 0.f;
#pragma unroll
    for (int k = 0; k < 8; ++k) { const f32x2 t = *reinterpret_cast<const f32x2*>(&L.upart[k][lq * 8 + (l15 & 3) * 2]); u0 += t[0]; u1 += t[1]; }
    u0 = bf16r(bf16r(u0) + bflo(b1r)); u1 = bf16r(bf16r(u1) + bfhi(b1r));
    const float z0 = 0.7978845608028654f * (u0 + 0.044715f * u0 * u0 * u0), z1 = 0.7978845608028654f * (u1 + 0.044715f * u1 * u1 * u1);
    const float th0 = 1.f - 2.f * __builtin_amdgcn_rcpf(__expf(2.f * z0) + 1.f), th1 = 1.f - 2.f * __builtin_amdgcn_rcpf(__expf(2.f * z1) + 1.f);
    const u32x4 ga = quad_gather(pk2(0.5f * u0 * (1.f + th0), 0.5f * u1 * (1.f + th1)));
    const f32x4 c0 = mfma(ga, f0, zero4), c1 = mfma(ga, f1, zero4), c2 = mfma(ga, f2, zero4), c3 = mfma(ga, f3, zero4);
    if (lane < 16) {
      u64* dst = h4 + (size_t)mb_ * 512 + wave * 64 + lane;
      put(dst, c0[0], tag); put(dst + 16, c1[0], tag); put(dst + 32, c2[0], tag); put(dst + 48, c3[0], tag);
    }
  }
  STAMP(8);
}

extern "C" void kernel_launch(void* const* d_in, const int* in_sizes, int n_in,
                              void* d_out, int out_size, void* d_ws, size_t ws_size,
                              hipStream_t stream) {
  const unsigned short* x = (const unsigned short*)d_in[0];
  const unsigned short* kc = (const unsigned short*)d_in[1];
  const unsigned short* vc = (const unsigned short*)d_in[2];
  const unsigned short* wq = (const unsigned short*)d_in[3];
  const unsigned short* wo = (const unsigned short*)d_in[4];
  const unsigned short* w1 = (const unsigned short*)d_in[6];
  const unsigned short* b1 = (const unsigned short*)d_in[7];
  const unsigned short* w2 = (const unsigned short*)d_in[8];
  const unsigned short* b2 = (const unsigned short*)d_in[9];
  fused_decode_kernel<<<NBLK, NTHR, 0, stream>>>(x, kc, vc, wq, wo, w1, b1, w2, b2,
                                                 (unsigned short*)d_out, (unsigned char*)d_ws);
}
```

```cpp
#include <hip/hip_runtime.h>
#include <hip/hip_bf16.h>
#include <stdint.h>

typedef unsigned long long u64;
typedef __attribute__((ext_vector_type(4))) unsigned u32x4;
typedef __attribute__((ext_vector_type(2))) unsigned u32x2;
typedef __attribute__((ext_vector_type(4))) float f32x4;
typedef __attribute__((ext_vector_type(2))) float f32x2;
typedef __attribute__((ext_vector_type(8))) short s16x8;

constexpr int DM = 512, HQ = 32, HKV = 8, SK = 65536, DH = 128, WIN = 4096, FF = 1792;
constexpr int NBLK = 256, NTHR = 512;
constexpr int NMLP = 56, NFIN = 8;

constexpr size_t WS_EPOCH = 0;
#define H1STRIDE (128 * 1024)
constexpr size_t WS_H1 = 4096;
constexpr size_t WS_H2O = WS_H1 + (size_t)8 * H1STRIDE;
constexpr int H2B = 528;
constexpr size_t WS_H3 = WS_H2O + (size_t)8 * 32 * H2B * 8;
constexpr size_t WS_H4 = WS_H3 + (size_t)16 * 512 * 8;
constexpr size_t WS_END = WS_H4 + (size_t)NMLP * 512 * 8;

constexpr unsigned SPIN_CAP = 1u << 18;
#define QDELAY 100
#define H2MODE 110
#define CODEPF 1
constexpr unsigned CODE_BYTES = 11904;
#define VEARLY 3

#define KV_NT 1

__device__ __forceinline__ float bf16r(float x) { return __bfloat162float(__float2bfloat16(x)); }
__device__ __forceinline__ float bflo(unsigned u) { return __uint_as_float(u << 16); }
__device__ __forceinline__ float bfhi(unsigned u) { return __uint_as_float(u & 0xffff0000u); }
__device__ __forceinline__ unsigned pk2(float lo, float hi) {
  return (__float_as_uint(bf16r(lo)) >> 16) | (__float_as_uint(bf16r(hi)) & 0xffff0000u);
}
__device__ __forceinline__ unsigned plo(unsigned a, unsigned b) { return __builtin_amdgcn_perm(b, a, 0x05040100u); }
__device__ __forceinline__ unsigned phi(unsigned a, unsigned b) { return __builtin_amdgcn_perm(b, a, 0x07060302u); }

template <int CTRL> __device__ __forceinline__ float dpp(float x) {
  return __builtin_bit_cast(float, __builtin_amdgcn_mov_dpp(__builtin_bit_cast(int, x), CTRL, 0xf, 0xf, true));
}
constexpr int XOR1 = 0xB1, XOR2 = 0x4E, ROR4 = 0x124, ROR8 = 0x128;
__device__ __forceinline__ float sw16_sum(float x) {
  auto s = __builtin_amdgcn_permlane16_swap(__float_as_uint(x), __float_as_uint(x), false, false);
  return __uint_as_float(s[0]) + __uint_as_float(s[1]);
}
__device__ __forceinline__ float sw32_sum(float x) {
  auto s = __builtin_amdgcn_permlane32_swap(__float_as_uint(x), __float_as_uint(x), false, false);
  return __uint_as_float(s[0]) + __uint_as_float(s[1]);
}
__device__ __forceinline__ float sw16_max(float x) {
  auto s = __builtin_amdgcn_permlane16_swap(__float_as_uint(x), __float_as_uint(x), false, false);
  return fmaxf(__uint_as_float(s[0]), __uint_as_float(s[1]));
}
__device__ __forceinline__ float sw32_max(float x) {
  auto s = __builtin_amdgcn_permlane32_swap(__float_as_uint(x), __float_as_uint(x), false, false);
  return fmaxf(__uint_as_float(s[0]), __uint_as_float(s[1]));
}
__device__ __forceinline__ float row16_sum(float x) {
  x += dpp<XOR1>(x); x += dpp<XOR2>(x); x += dpp<ROR4>(x); x += dpp<ROR8>(x); return x;
}
__device__ __forceinline__ float row16_max(float x) {
  x = fmaxf(x, dpp<XOR1>(x)); x = fmaxf(x, dpp<XOR2>(x)); x = fmaxf(x, dpp<ROR4>(x)); x = fmaxf(x, dpp<ROR8>(x)); return x;
}
template <int CTRL> __device__ __forceinline__ unsigned dppu(unsigned x) {
  return (unsigned)__builtin_amdgcn_mov_dpp((int)x, CTRL, 0xf, 0xf, true);
}
__device__ __forceinline__ u32x4 quad_gather(unsigned x) {
  return u32x4{dppu<0x00>(x), dppu<0x55>(x), dppu<0xAA>(x), dppu<0xFF>(x)};
}
__device__ __forceinline__ float wave_sum(float x) { return sw32_sum(sw16_sum(row16_sum(x))); }
__device__ __forceinline__ f32x4 mfma(u32x4 a, u32x4 b, f32x4 c) {
  return __builtin_amdgcn_mfma_f32_16x16x32_bf16(__builtin_bit_cast(s16x8, a), __builtin_bit_cast(s16x8, b), c, 0, 0, 0);
}

template <typename T> __device__ __forceinline__ T ldw(const void* p) {
  return __builtin_nontemporal_load(reinterpret_cast<const T*>(p));
}

__device__ __forceinline__ void put(u64* g, float v, unsigned tag) {
  __hip_atomic_store(g, ((u64)tag << 32) | (__float_as_uint(v) & 0xffffff00u), __ATOMIC_RELAXED, __HIP_MEMORY_SCOPE_AGENT);
}
__device__ __forceinline__ u64 get(const u64* g) {
  return __hip_atomic_load(g, __ATOMIC_RELAXED, __HIP_MEMORY_SCOPE_AGENT);
}
__device__ __forceinline__ float gval(u64 x) { return __uint_as_float((unsigned)x); }
__device__ __forceinline__ u32x4 get2(__amdgpu_buffer_rsrc_t r, unsigned off) {
  asm volatile("" ::: "memory");
  return __builtin_amdgcn_raw_buffer_load_b128(r, (int)off, 0, 16);
}
template <int N> __device__ __forceinline__ bool gall2(const u32x4 (&v)[N], unsigned tag) {
  bool ok = true; unsigned lo = 0;
#pragma unroll
  for (int k = 0; k < N; ++k) { ok &= (v[k][1] == tag) & (v[k][3] == tag); lo |= v[k][0] | v[k][2]; }
  ok &= (lo & 0xffu) == 0u;
  return __all(ok);
}
template <int N> __device__ __forceinline__ bool gall(const u64 (&v)[N], unsigned tag) {
  bool ok = true; unsigned lo = 0;
#pragma unroll
  for (int k = 0; k < N; ++k) { ok &= (unsigned)(v[k] >> 32) == tag; lo |= (unsigned)v[k]; }
  ok &= (lo & 0xffu) == 0u;
  return __all(ok);
}

__device__ __forceinline__ bool gok2(u32x4 v, unsigned tag) {
  return __all((v[1] == tag) & (v[3] == tag) & (((v[0] | v[2]) & 0xffu) == 0u));
}
__device__ __forceinline__ void bar() {
  asm volatile("s_waitcnt lgkmcnt(0)" ::: "memory");
  __builtin_amdgcn_s_barrier();
  asm volatile("" ::: "memory");
}
__device__ __forceinline__ void lds_fence() { asm volatile("s_waitcnt lgkmcnt(0)" ::: "memory"); }
#define PIN() do { asm volatile("" ::: "memory"); __builtin_amdgcn_sched_barrier(0); } while (0)

#define STAMP(k) do { } while (0)

struct Lds {
  float qpart[4][64];
  unsigned qs[4][64];
  float wmax[4][4];
  float wsum[4][4];
  float opart[4][512];
  float wgt[8][32];
  unsigned short orow[8][32];
  float wpart[8][64];
  float hs[512];
  float hstat[8][2];
  unsigned short hn[8][64];
  float upart[8][32];
  unsigned short gs[8][32];
  float fpart[8][64];
  unsigned tag[4];
  unsigned qcnt[4];
  float pad[18 * 1024];
};

__global__ void __launch_bounds__(NTHR, 2)
fused_decode_kernel(const unsigned short* __restrict__ x, const unsigned short* __restrict__ kc,
                    const unsigned short* __restrict__ vc, const unsigned short* __restrict__ wq,
                    const unsigned short* __restrict__ wo, const unsigned short* __restrict__ w1,
                    const unsigned short* __restrict__ b1, const unsigned short* __restrict__ w2,
                    const unsigned short* __restrict__ b2, unsigned short* __restrict__ out,
                    unsigned char* ws) {
  __shared__ __attribute__((aligned(16))) Lds L;
  unsigned cp0;
  {
    u64 pc0;
    asm volatile("s_getpc_b64 %0" : "=s"(pc0));
    typedef const __attribute__((address_space(1))) unsigned* gptr;
    const unsigned o0 = threadIdx.x * 128u;
    cp0 = *(gptr)((pc0 & ~(u64)255) + (o0 < CODE_BYTES ? o0 : 0u));
  }
  asm volatile("" :: "s"(x), "s"(kc), "s"(vc), "s"(wq), "s"(wo), "s"(w1), "s"(b1), "s"(w2), "s"(b2), "s"(out), "s"(ws));
  const int tid = threadIdx.x, lane = tid & 63, l15 = lane & 15, lq = lane >> 4;
  const int wave = __builtin_amdgcn_readfirstlane(tid >> 6);
  const int b = blockIdx.x, g = b & 7, i = b >> 3;
  u64* const h1 = (u64*)(ws + WS_H1);
  u64* const h2o = (u64*)(ws + WS_H2O);
  u64* const h3 = (u64*)(ws + WS_H3);
  u64* const h4 = (u64*)(ws + WS_H4);
  const __amdgpu_buffer_rsrc_t wsr = __builtin_amdgcn_make_buffer_rsrc(ws, 0, (int)WS_END, 0x00020000);
  const f32x4 zero4 = {0.f, 0.f, 0.f, 0.f};
  const u32x4 zerou = {0u, 0u, 0u, 0u};
  unsigned tag;
  const int rb = 2 * g + ((i >> 3) & 1), cb = i & 7;
  const int mb_ = (i - 16) * 8 + g;
  unsigned w1v[16];
  u32x2 w2v[8];
  STAMP(0);

  if (__builtin_expect(wave < 4, 1)) {
    const int a = wave, kr = i >> 3, cq = i & 7;
    const u32x4 xv = *reinterpret_cast<const u32x4*>(x + lane * 8);
    const unsigned ep = __hip_atomic_load(reinterpret_cast<const unsigned*>(ws + WS_EPOCH), __ATOMIC_RELAXED, __HIP_MEMORY_SCOPE_AGENT);
    u32x2 wqv[8];
    {
      const __amdgpu_buffer_rsrc_t wqr = __builtin_amdgcn_make_buffer_rsrc(const_cast<unsigned short*>(wq), 0, DM * HQ * DH * 2, 0x00020000);
      const int vo = (lq * 8 * (HQ * DH) + l15 * 4) * 2, so = ((kr * 128 + a * 32) * (HQ * DH) + g * 512 + cq * 64) * 2;
#pragma unroll
      for (int j = 0; j < 8; ++j) wqv[j] = __builtin_amdgcn_raw_buffer_load_b64(wqr, vo, so + j * (HQ * DH * 2), 2);
    }
    if (tid == 0) L.qcnt[0] = 0u;
    PIN();
    bar();
    PIN();
    float xf[8];
#pragma unroll
    for (int k = 0; k < 4; ++k) { xf[2 * k] = bflo(xv[k]); xf[2 * k + 1] = bfhi(xv[k]); }
    float s1 = 0.f;
#pragma unroll
    for (int k = 0; k < 8; ++k) s1 += xf[k];
    const float mean = wave_sum(s1) * (1.f / DM);
    float s2 = 0.f;
#pragma unroll
    for (int k = 0; k < 8; ++k) { const float d = xf[k] - mean; s2 += d * d; }
    const float var = wave_sum(s2) * (1.f / DM);
    const float mb = bf16r(mean), rden = __builtin_amdgcn_rcpf(bf16r(__builtin_amdgcn_sqrtf(bf16r(bf16r(var) + 1e-5f))));
    u32x4 xn;
#pragma unroll
    for (int k = 0; k < 4; ++k) xn[k] = pk2(bf16r(xf[2 * k] - mb) * rden, bf16r(xf[2 * k + 1] - mb) * rden);
    u32x4 xa;
    {
      const int src = (kr * 16 + a * 4 + lq) * 4;
#pragma unroll
      for (int k = 0; k < 4; ++k) xa[k] = (unsigned)__builtin_amdgcn_ds_bpermute(src, (int)xn[k]);
    }
    tag = ep + 1u;
    if (tid == 0) L.tag[0] = tag;
    {
      f32x4 qc[4];
      u32x4 f0, f1, f2, f3;
#pragma unroll
      for (int k = 0; k < 4; ++k) {
        f0[k] = plo(wqv[2 * k][0], wqv[2 * k + 1][0]); f1[k] = phi(wqv[2 * k][0], wqv[2 * k + 1][0]);
        f2[k] = plo(wqv[2 * k][1], wqv[2 * k + 1][1]); f3[k] = phi(wqv[2 * k][1], wqv[2 * k + 1][1]);
      }
      qc[0] = mfma(xa, f0, zero4); qc[1] = mfma(xa, f1, zero4); qc[2] = mfma(xa, f2, zero4); qc[3] = mfma(xa, f3, zero4);
      if (lane < 16) *reinterpret_cast<f32x4*>(&L.qpart[a][lane * 4]) = f32x4{qc[0][0], qc[1][0], qc[2][0], qc[3][0]};
    }
    STAMP(1);
    lds_fence();
    if (a != 0) {
      if (lane == 0) __hip_atomic_fetch_add(&L.qcnt[0], 1u, __ATOMIC_RELAXED, __HIP_MEMORY_SCOPE_WORKGROUP);
    } else {
      for (unsigned spins = 0; spins < SPIN_CAP; ++spins) {
        if (__hip_atomic_load(&L.qcnt[0], __ATOMIC_RELAXED, __HIP_MEMORY_SCOPE_WORKGROUP) == 3u) break;
      }
      asm volatile("" ::: "memory");
      const float qv = (L.qpart[0][lane] + L.qpart[1][lane]) + (L.qpart[2][lane] + L.qpart[3][lane]);
      put(h1 + g * (H1STRIDE / 8) + kr * 512 + cq * 64 + lane, qv, tag);
    }
    PIN();
    bar();
    PIN();
    STAMP(2);
    {
      const unsigned src = (unsigned)WS_H1 + g * H1STRIDE + (a * 128 + lane * 2) * 8;
      u32x4 v[4];
      {
        const u64 t0 = __builtin_amdgcn_s_memrealtime();
        while (__builtin_amdgcn_s_memrealtime() - t0 < QDELAY) __builtin_amdgcn_s_sleep(1);
      }
      PIN();
#pragma unroll
      for (int k = 0; k < 4; ++k) v[k] = get2(wsr, src + k * 4096);
      PIN();
      bar();
      PIN();
      for (unsigned spins = 0; spins < SPIN_CAP; ++spins) {
        if (gall2(v, tag)) break;
#pragma unroll
        for (int k = 0; k < 4; ++k) v[k] = get2(wsr, src + k * 4096);
      }
      const float q0 = (__uint_as_float(v[0][0]) + __uint_as_float(v[1][0])) + (__uint_as_float(v[2][0]) + __uint_as_float(v[3][0]));
      const float q1 = (__uint_as_float(v[0][2]) + __uint_as_float(v[1][2])) + (__uint_as_float(v[2][2]) + __uint_as_float(v[3][2]));
      L.qs[a][lane] = pk2(q0, q1);
    }
    asm volatile("" :: "v"(cp0));
    STAMP(3);
    bar();
    PIN();
    PIN();
    bar();
    bar();
  } else {
    const int w4 = wave - 4;
    const size_t kvbase = ((size_t)g * SK + (SK - WIN) + i * 128 + w4 * 32) * DH;
    bar();
    PIN();
    u32x4 kf[2][4];
    u32x4 vv[8];
#pragma unroll
    for (int c = 0; c < 4; ++c) kf[0][c] = ldw<u32x4>(kc + kvbase + (size_t)l15 * DH + c * 32 + lq * 8);
    PIN();
    bar();
    PIN();
#pragma unroll
    for (int c = 0; c < 4; ++c) kf[1][c] = ldw<u32x4>(kc + kvbase + (size_t)(16 + l15) * DH + c * 32 + lq * 8);
#pragma unroll
    for (int j = 0; j < VEARLY; ++j) vv[j] = ldw<u32x4>(vc + kvbase + (size_t)((j >> 2) * 16 + lq * 4 + (j & 3)) * DH + l15 * 8);
    PIN();
    bar();
    PIN();
#pragma unroll
    for (int j = VEARLY; j < 8; ++j) vv[j] = ldw<u32x4>(vc + kvbase + (size_t)((j >> 2) * 16 + lq * 4 + (j & 3)) * DH + l15 * 8);
    PIN();
    tag = L.tag[0];
    bar();
    asm volatile("" :: "v"(cp0));
    STAMP(1);
    f32x4 s0 = zero4, s1 = zero4;
    {
      const unsigned* qsrc = &L.qs[l15 & 3][lq * 4];
      u32x4 qa[4];
#pragma unroll
      for (int c = 0; c < 4; ++c) qa[c] = *reinterpret_cast<const u32x4*>(qsrc + c * 16);
#pragma unroll
      for (int c = 0; c < 4; ++c) { s0 = mfma(kf[0][c], qa[c], s0); s1 = mfma(kf[1][c], qa[c], s1); }
    }
    float sc[8];
#pragma unroll
    for (int r = 0; r < 4; r += 2) {
      unsigned t = pk2(s0[r], s0[r + 1]);
      t = pk2(bflo(t) * 0.08838834764831845f, bfhi(t) * 0.08838834764831845f);
      sc[r] = bflo(t); sc[r + 1] = bfhi(t);
      t = pk2(s1[r], s1[r + 1]);
      t = pk2(bflo(t) * 0.08838834764831845f, bfhi(t) * 0.08838834764831845f);
      sc[4 + r] = bflo(t); sc[5 + r] = bfhi(t);
    }
    {
      float mx = fmaxf(fmaxf(fmaxf(sc[0], sc[1]), fmaxf(sc[2], sc[3])), fmaxf(fmaxf(sc[4], sc[5]), fmaxf(sc[6], sc[7])));
      mx = sw32_max(sw16_max(mx));
      if (lane < 4) L.wmax[lane][w4] = mx;
    }
    STAMP(2);
    bar();
    u32x4 pb;
    {
      const f32x4 m4 = *reinterpret_cast<const f32x4*>(&L.wmax[l15 & 3][0]);
      const float M = fmaxf(fmaxf(m4[0], m4[1]), fmaxf(m4[2], m4[3]));
      float pp[8];
#pragma unroll
      for (int r = 0; r < 8; ++r) pp[r] = __expf(sc[r] - M);
      float ls = ((pp[0] + pp[1]) + (pp[2] + pp[3])) + ((pp[4] + pp[5]) + (pp[6] + pp[7]));
      ls = sw32_sum(sw16_sum(ls));
      if (lane < 4) L.wsum[lane][w4] = ls;
#pragma unroll
      for (int r = 0; r < 4; ++r) pb[r] = pk2(pp[2 * r], pp[2 * r + 1]);
    }
    f32x4 oc[8];
#pragma unroll
    for (int c2 = 0; c2 < 4; ++c2) {
      u32x4 f0, f1;
#pragma unroll
      for (int k = 0; k < 4; ++k) { f0[k] = plo(vv[2 * k][c2], vv[2 * k + 1][c2]); f1[k] = phi(vv[2 * k][c2], vv[2 * k + 1][c2]); }
      oc[2 * c2] = mfma(f0, pb, zero4);
      oc[2 * c2 + 1] = mfma(f1, pb, zero4);
    }
    if (l15 < 4) {
#pragma unroll
      for (int r = 0; r < 4; ++r) {
        float* dst = &L.opart[w4][l15 * 128 + (lq * 4 + r) * 8];
        *reinterpret_cast<f32x4*>(dst) = f32x4{oc[0][r], oc[1][r], oc[2][r], oc[3][r]};
        *reinterpret_cast<f32x4*>(dst + 4) = f32x4{oc[4][r], oc[5][r], oc[6][r], oc[7][r]};
      }
    }
    STAMP(3);
    bar();
  }

  {
    const float o = (L.opart[0][tid] + L.opart[1][tid]) + (L.opart[2][tid] + L.opart[3][tid]);
    put(h2o + ((size_t)g * 32 + i) * H2B + tid, o, tag);
    if (tid < 4) {
      const float l = (L.wsum[tid][0] + L.wsum[tid][1]) + (L.wsum[tid][2] + L.wsum[tid][3]);
      const float m = fmaxf(fmaxf(L.wmax[tid][0], L.wmax[tid][1]), fmaxf(L.wmax[tid][2], L.wmax[tid][3]));
      put(h2o + ((size_t)g * 32 + i) * H2B + 512 + tid * 2, m, tag);
      put(h2o + ((size_t)g * 32 + i) * H2B + 513 + tid * 2, l, tag);
    }
  }
  STAMP(4);
  if (i < 16) {
    PIN();
    u32x2 wov[8];
#pragma unroll
    for (int j = 0; j < 8; ++j)
      wov[j] = ldw<u32x2>(wo + (size_t)(rb * 256 + wave * 32 + lq * 8 + j) * DM + cb * 64 + l15 * 4);
    PIN();
    const int hl = (rb & 1) * 2 + (wave >> 2);
    const unsigned so = (unsigned)WS_H2O + ((g * 32 + (l15 >> 2) * 8) * H2B + hl * 128 + (wave & 3) * 32 + lq * 8 + (l15 & 3) * 2) * 8;
    const unsigned ss = (unsigned)WS_H2O + ((g * 32 + (lane & 31)) * H2B + 512 + hl * 2) * 8;
    u32x4 v[9];
    {
      const u64 t0 = __builtin_amdgcn_s_memrealtime();
      while (__builtin_amdgcn_s_memrealtime() - t0 < H2MODE) __builtin_amdgcn_s_sleep(1);
    }
    PIN();
    v[8] = get2(wsr, ss);
#pragma unroll
    for (int k = 0; k < 8; ++k) v[k] = get2(wsr, so + k * (H2B * 8));
    PIN();
    u32x4 f0, f1, f2, f3;
#pragma unroll
    for (int k = 0; k < 4; ++k) {
      f0[k] = plo(wov[2 * k][0], wov[2 * k + 1][0]); f1[k] = phi(wov[2 * k][0], wov[2 * k + 1][0]);
      f2[k] = plo(wov[2 * k][1], wov[2 * k + 1][1]); f3[k] = phi(wov[2 * k][1], wov[2 * k + 1][1]);
    }
    asm volatile("" : "+v"(f0), "+v"(f1), "+v"(f2), "+v"(f3));
    if (!gall2(v, tag)) {
      for (unsigned spins = 0; spins < SPIN_CAP; ++spins) {
        bool all = true;
        if (!gok2(v[8], tag)) { all = false; v[8] = get2(wsr, ss); }
#pragma unroll
        for (int k = 0; k < 8; ++k) { if (!gok2(v[k], tag)) { all = false; v[k] = get2(wsr, so + k * (H2B * 8)); } }
        if (all) break;
      }
    }
    STAMP(5);
    const float mm = __uint_as_float(v[8][0]);
    float M = sw16_max(row16_max(mm));
    const float wg = __expf(mm - M);
    if (lane < 32) L.wgt[wave][lane] = wg;
    const float Ls = sw16_sum(row16_sum(wg * __uint_as_float(v[8][2])));
    lds_fence();
    float o0 = 0.f, o1 = 0.f;
#pragma unroll
    for (int k4 = 0; k4 < 2; ++k4) {
      const f32x4 wv = *reinterpret_cast<const f32x4*>(&L.wgt[wave][(l15 >> 2) * 8 + k4 * 4]);
#pragma unroll
      for (int k = 0; k < 4; ++k) { o0 = fmaf(wv[k], __uint_as_float(v[k4 * 4 + k][0]), o0); o1 = fmaf(wv[k], __uint_as_float(v[k4 * 4 + k][2]), o1); }
    }
    o0 += dpp<ROR4>(o0); o0 += dpp<ROR8>(o0); o1 += dpp<ROR4>(o1); o1 += dpp<ROR8>(o1);
    const float rl = __builtin_amdgcn_rcpf(Ls);
    const u32x4 oa = quad_gather(pk2(o0 * rl, o1 * rl));
    const f32x4 c0 = mfma(oa, f0, zero4), c1 = mfma(oa, f1, zero4), c2 = mfma(oa, f2, zero4), c3 = mfma(oa, f3, zero4);
    if (lane < 16) *reinterpret_cast<f32x4*>(&L.wpart[wave][lane * 4]) = f32x4{c0[0], c1[0], c2[0], c3[0]};
    bar();
    if (tid < 64) {
      float t = 0.f;
#pragma unroll
      for (int k = 0; k < 8; ++k) t += L.wpart[k][tid];
      put(h3 + (size_t)rb * 512 + cb * 64 + tid, t, tag);
    }
    STAMP(6);
    return;
  }
  if (mb_ >= NMLP + NFIN) return;
  if (mb_ >= NMLP) {
    const int fb = mb_ - NMLP;
    const unsigned xr = *reinterpret_cast<const unsigned*>(x + fb * 64 + (lane & 31) * 2);
    const unsigned short b2r = b2[fb * 64 + lane];
    if (wave == 0) {
      const unsigned src = (unsigned)WS_H3 + ((lane >> 5) * 8 * 512 + fb * 64 + (lane & 31) * 2) * 8;
      u32x4 v[8];
      for (unsigned spins = 0; spins < SPIN_CAP; ++spins) {
#pragma unroll
        for (int k = 0; k < 8; ++k) v[k] = get2(wsr, src + k * 4096);
        if (gall2(v, tag)) break;
        __builtin_amdgcn_s_sleep(1);
      }
      float t0 = 0.f, t1 = 0.f;
#pragma unroll
      for (int k = 0; k < 8; ++k) { t0 += __uint_as_float(v[k][0]); t1 += __uint_as_float(v[k][2]); }
      t0 = sw32_sum(t0); t1 = sw32_sum(t1);
      if (lane < 32) *reinterpret_cast<f32x2*>(&L.hs[lane * 2]) = f32x2{bf16r(bflo(xr) + bf16r(t0)), bf16r(bfhi(xr) + bf16r(t1))};
    }
    {
      u32x4 v[4];
      const int p0 = wave * 7 + (lane >> 5) * 4;
      const unsigned base = (unsigned)WS_H4 + (p0 * 512 + fb * 64 + (lane & 31) * 2) * 8;
      const unsigned last = (lane >> 5) ? 2u * 4096u : 3u * 4096u;
      for (unsigned spins = 0; spins < SPIN_CAP; ++spins) {
#pragma unroll
        for (int k = 0; k < 3; ++k) v[k] = get2(wsr, base + k * 4096);
        v[3] = get2(wsr, base + last);
        if (gall2(v, tag)) break;
        __builtin_amdgcn_s_sleep(1);
      }
      const float w3 = (lane >> 5) ? 0.f : 1.f;
      float t0 = (__uint_as_float(v[0][0]) + __uint_as_float(v[1][0])) + (__uint_as_float(v[2][0]) + w3 * __uint_as_float(v[3][0]));
      float t1 = (__uint_as_float(v[0][2]) + __uint_as_float(v[1][2])) + (__uint_as_float(v[2][2]) + w3 * __uint_as_float(v[3][2]));
      t0 = sw32_sum(t0); t1 = sw32_sum(t1);
      if (lane < 32) *reinterpret_cast<f32x2*>(&L.fpart[wave][lane * 2]) = f32x2{t0, t1};
    }
    STAMP(9);
    bar();
    if (tid < 64) {
      const int slot = (tid & 3) * 16 + (tid >> 2);
      float t = 0.f;
#pragma unroll
      for (int k = 0; k < 8; ++k) t += L.fpart[k][slot];
      const float y = bf16r(bf16r(t) + bflo(b2r));
      const float r = bf16r(L.hs[tid] + y);
      out[fb * 64 + tid] = (unsigned short)(__float_as_uint(r) >> 16);
      if (fb == 0 && tid == 0) *reinterpret_cast<unsigned*>(ws + WS_EPOCH) = tag;
    }
    STAMP(10);
    return;
  }

  PIN();
  {
#pragma unroll
    for (int j = 0; j < 16; ++j)
      w1v[j] = ldw<unsigned>(w1 + (size_t)(wave * 64 + (j >> 3) * 32 + lq * 8 + (j & 7)) * FF + mb_ * 32 + l15 * 2);
#pragma unroll
    for (int j = 0; j < 8; ++j)
      w2v[j] = ldw<u32x2>(w2 + (size_t)(mb_ * 32 + lq * 8 + j) * DM + wave * 64 + l15 * 4);
  }
  const int cp = ((l15 >> 2) & 1) * 16 + lq * 4 + (l15 & 3);
  const unsigned xr = *reinterpret_cast<const unsigned*>(x + wave * 64 + cp * 2);
  const unsigned b1r = *reinterpret_cast<const unsigned*>(b1 + mb_ * 32 + lq * 8 + (l15 & 3) * 2);
  PIN();
  u32x4 e0[2], e1[2], f0, f1, f2, f3;
#pragma unroll
  for (int ks = 0; ks < 2; ++ks)
#pragma unroll
    for (int k = 0; k < 4; ++k) { e0[ks][k] = plo(w1v[ks * 8 + 2 * k], w1v[ks * 8 + 2 * k + 1]); e1[ks][k] = phi(w1v[ks * 8 + 2 * k], w1v[ks * 8 + 2 * k + 1]); }
#pragma unroll
  for (int k = 0; k < 4; ++k) {
    f0[k] = plo(w2v[2 * k][0], w2v[2 * k + 1][0]); f1[k] = phi(w2v[2 * k][0], w2v[2 * k + 1][0]);
    f2[k] = plo(w2v[2 * k][1], w2v[2 * k + 1][1]); f3[k] = phi(w2v[2 * k][1], w2v[2 * k + 1][1]);
  }
  PIN();
  asm volatile("" : "+v"(e0[0]), "+v"(e0[1]), "+v"(e1[0]), "+v"(e1[1]), "+v"(f0), "+v"(f1), "+v"(f2), "+v"(f3));
  float hv0, hv1;
  {
    const unsigned src = (unsigned)WS_H3 + ((l15 >> 3) * 8 * 512 + wave * 64 + cp * 2) * 8;
    u32x4 v[8];
    for (unsigned spins = 0; spins < SPIN_CAP; ++spins) {
#pragma unroll
      for (int k = 0; k < 8; ++k) v[k] = get2(wsr, src + k * 4096);
      if (gall2(v, tag)) break;
      __builtin_amdgcn_s_sleep(1);
    }
    float t0 = 0.f, t1 = 0.f;
#pragma unroll
    for (int k = 0; k < 8; ++k) { t0 += __uint_as_float(v[k][0]); t1 += __uint_as_float(v[k][2]); }
    t0 += dpp<ROR8>(t0); t1 += dpp<ROR8>(t1);
    hv0 = bf16r(bflo(xr) + bf16r(t0));
    hv1 = bf16r(bfhi(xr) + bf16r(t1));
    const float a1 = wave_sum(hv0 + hv1), a2 = wave_sum(hv0 * hv0 + hv1 * hv1);
    if (lane == 0) *reinterpret_cast<f32x2*>(&L.hstat[wave][0]) = f32x2{a1, a2};
  }
  STAMP(7);
  bar();
  {
    float t1 = 0.f, t2 = 0.f;
#pragma unroll
    for (int k = 0; k < 4; ++k) { const f32x4 t = *reinterpret_cast<const f32x4*>(&L.hstat[2 * k][0]); t1 += t[0] + t[2]; t2 += t[1] + t[3]; }
    const float mean2 = t1 * (0.5f / DM);
    const float var2 = fmaxf(t2 * (0.5f / DM) - mean2 * mean2, 0.f);
    const float mb2 = bf16r(mean2), rden2 = __builtin_amdgcn_rcpf(bf16r(__builtin_amdgcn_sqrtf(bf16r(bf16r(var2) + 1e-5f))));
    const u32x4 ha = quad_gather(pk2(bf16r(hv0 - mb2) * rden2, bf16r(hv1 - mb2) * rden2));
    const f32x4 c0a = mfma(ha, e0[0], zero4), c1a = mfma(ha, e1[0], zero4), c0b = mfma(ha, e0[1], zero4), c1b = mfma(ha, e1[1], zero4);
    const float u0 = sw16_sum(lane < 16 ? c0a[0] : c0b[0]), u1 = sw16_sum(lane < 16 ? c1a[0] : c1b[0]);
    if (lane < 16) *reinterpret_cast<f32x2*>(&L.upart[wave][lane * 2]) = f32x2{u0, u1};
  }
  bar();
  {
    float u0 = 0.f, u1 = 0.f;
#pragma unroll
    for (int k = 0; k < 8; ++k) { const f32x2 t = *reinterpret_cast<const f32x2*>(&L.upart[k][lq * 8 + (l15 & 3) * 2]); u0 += t[0]; u1 += t[1]; }
    u0 = bf16r(bf16r(u0) + bflo(b1r)); u1 = bf16r(bf16r(u1) + bfhi(b1r));
    const float z0 = 0.7978845608028654f * (u0 + 0.044715f * u0 * u0 * u0), z1 = 0.7978845608028654f * (u1 + 0.044715f * u1 * u1 * u1);
    const float th0 = 1.f - 2.f * __builtin_amdgcn_rcpf(__expf(2.f * z0) + 1.f), th1 = 1.f - 2.f * __builtin_amdgcn_rcpf(__expf(2.f * z1) + 1.f);
    const u32x4 ga = quad_gather(pk2(0.5f * u0 * (1.f + th0), 0.5f * u1 * (1.f + th1)));
    const f32x4 c0 = mfma(ga, f0, zero4), c1 = mfma(ga, f1, zero4), c2 = mfma(ga, f2, zero4), c3 = mfma(ga, f3, zero4);
    if (lane < 16) {
      u64* dst = h4 + (size_t)mb_ * 512 + wave * 64 + lane;
      put(dst, c0[0], tag); put(dst + 16, c1[0], tag); put(dst + 32, c2[0], tag); put(dst + 48, c3[0], tag);
    }
  }
  STAMP(8);
}

extern "C" void kernel_launch(void* const* d_in, const int* in_sizes, int n_in,
                              void* d_out, int out_size, void* d_ws, size_t ws_size,
                              hipStream_t stream) {
  const unsigned short* x = (const unsigned short*)d_in[0];
  const unsigned short* kc = (const unsigned short*)d_in[1];
  const unsigned short* vc = (const unsigned short*)d_in[2];
  const unsigned short* wq = (const unsigned short*)d_in[3];
  const unsigned short* wo = (const unsigned short*)d_in[4];
  const unsigned short* w1 = (const unsigned short*)d_in[6];
  const unsigned short* b1 = (const unsigned short*)d_in[7];
  const unsigned short* w2 = (const unsigned short*)d_in[8];
  const unsigned short* b2 = (const unsigned short*)d_in[9];
  fused_decode_kernel<<<NBLK, NTHR, 0, stream>>>(x, kc, vc, wq, wo, w1, b1, w2, b2,
                                                 (unsigned short*)d_out, (unsigned char*)d_ws);
}
```

```cpp
#include <hip/hip_runtime.h>
#include <hip/hip_bf16.h>
#include <stdint.h>

typedef unsigned long long u64;
typedef __attribute__((ext_vector_type(4))) unsigned u32x4;
typedef __attribute__((ext_vector_type(2))) unsigned u32x2;
typedef __attribute__((ext_vector_type(4))) float f32x4;
typedef __attribute__((ext_vector_type(2))) float f32x2;
typedef __attribute__((ext_vector_type(8))) short s16x8;

constexpr int DM = 512, HQ = 32, HKV = 8, SK = 65536, DH = 128, WIN = 4096, FF = 1792;
constexpr int NBLK = 256, NTHR = 512;
constexpr int NMLP = 56, NFIN = 8;

constexpr size_t WS_EPOCH = 0;
constexpr unsigned H1STRIDE = 128 * 1024;
constexpr size_t WS_H1 = 4096;
constexpr size_t WS_H2O = WS_H1 + (size_t)8 * H1STRIDE;
constexpr int H2B = 528;
constexpr size_t WS_H3 = WS_H2O + (size_t)8 * 32 * H2B * 8;
constexpr size_t WS_H4 = WS_H3 + (size_t)16 * 512 * 8;
constexpr size_t WS_END = WS_H4 + (size_t)NMLP * 512 * 8;

constexpr unsigned SPIN_CAP = 1u << 18;
#define QDELAY 100
#define H2MODE 110
constexpr unsigned CODE_BYTES = 11264;
#define VEARLY 3

__device__ __forceinline__ float bf16r(float x) { return __bfloat162float(__float2bfloat16(x)); }
__device__ __forceinline__ float bflo(unsigned u) { return __uint_as_float(u << 16); }
__device__ __forceinline__ float bfhi(unsigned u) { return __uint_as_float(u & 0xffff0000u); }
__device__ __forceinline__ unsigned pk2(float lo, float hi) {
  return (__float_as_uint(bf16r(lo)) >> 16) | (__float_as_uint(bf16r(hi)) & 0xffff0000u);
}
__device__ __forceinline__ unsigned plo(unsigned a, unsigned b) { return __builtin_amdgcn_perm(b, a, 0x05040100u); }
__device__ __forceinline__ unsigned phi(unsigned a, unsigned b) { return __builtin_amdgcn_perm(b, a, 0x07060302u); }

template <int CTRL> __device__ __forceinline__ float dpp(float x) {
  return __builtin_bit_cast(float, __builtin_amdgcn_mov_dpp(__builtin_bit_cast(int, x), CTRL, 0xf, 0xf, true));
}
constexpr int XOR1 = 0xB1, XOR2 = 0x4E, ROR4 = 0x124, ROR8 = 0x128;
__device__ __forceinline__ float sw16_sum(float x) {
  auto s = __builtin_amdgcn_permlane16_swap(__float_as_uint(x), __float_as_uint(x), false, false);
  return __uint_as_float(s[0]) + __uint_as_float(s[1]);
}
__device__ __forceinline__ float sw32_sum(float x) {
  auto s = __builtin_amdgcn_permlane32_swap(__float_as_uint(x), __float_as_uint(x), false, false);
  return __uint_as_float(s[0]) + __uint_as_float(s[1]);
}
__device__ __forceinline__ float sw16_max(float x) {
  auto s = __builtin_amdgcn_permlane16_swap(__float_as_uint(x), __float_as_uint(x), false, false);
  return fmaxf(__uint_as_float(s[0]), __uint_as_float(s[1]));
}
__device__ __forceinline__ float sw32_max(float x) {
  auto s = __builtin_amdgcn_permlane32_swap(__float_as_uint(x), __float_as_uint(x), false, false);
  return fmaxf(__uint_as_float(s[0]), __uint_as_float(s[1]));
}
__device__ __forceinline__ float row16_sum(float x) {
  x += dpp<XOR1>(x); x += dpp<XOR2>(x); x += dpp<ROR4>(x); x += dpp<ROR8>(x); return x;
}
__device__ __forceinline__ float row16_max(float x) {
  x = fmaxf(x, dpp<XOR1>(x)); x = fmaxf(x, dpp<XOR2>(x)); x = fmaxf(x, dpp<ROR4>(x)); x = fmaxf(x, dpp<ROR8>(x)); return x;
}
template <int CTRL> __device__ __forceinline__ unsigned dppu(unsigned x) {
  return (unsigned)__builtin_amdgcn_mov_dpp((int)x, CTRL, 0xf, 0xf, true);
}
__device__ __forceinline__ u32x4 quad_gather(unsigned x) {
  return u32x4{dppu<0x00>(x), dppu<0x55>(x), dppu<0xAA>(x), dppu<0xFF>(x)};
}
__device__ __forceinline__ float wave_sum(float x) { return sw32_sum(sw16_sum(row16_sum(x))); }
__device__ __forceinline__ f32x4 mfma(u32x4 a, u32x4 b, f32x4 c) {
  return __builtin_amdgcn_mfma_f32_16x16x32_bf16(__builtin_bit_cast(s16x8, a), __builtin_bit_cast(s16x8, b), c, 0, 0, 0);
}

template <typename T> __device__ __forceinline__ T ldw(const void* p) {
  return __builtin_nontemporal_load(reinterpret_cast<const T*>(p));
}

__device__ __forceinline__ void put(u64* g, float v, unsigned tag) {
  __hip_atomic_store(g, ((u64)tag << 32) | (__float_as_uint(v) & 0xffffff00u), __ATOMIC_RELAXED, __HIP_MEMORY_SCOPE_AGENT);
}
__device__ __forceinline__ u32x4 get2(__amdgpu_buffer_rsrc_t r, unsigned off) {
  asm volatile("" ::: "memory");
  return __builtin_amdgcn_raw_buffer_load_b128(r, (int)off, 0, 16);
}
template <int N> __device__ __forceinline__ bool gall2(const u32x4 (&v)[N], unsigned tag) {
  bool ok = true; unsigned lo = 0;
#pragma unroll
  for (int k = 0; k < N; ++k) { ok &= (v[k][1] == tag) & (v[k][3] == tag); lo |= v[k][0] | v[k][2]; }
  ok &= (lo & 0xffu) == 0u;
  return __all(ok);
}
__device__ __forceinline__ bool gok2(u32x4 v, unsigned tag) {
  return __all((v[1] == tag) & (v[3] == tag) & (((v[0] | v[2]) & 0xffu) == 0u));
}
__device__ __forceinline__ void bar() {
  asm volatile("s_waitcnt lgkmcnt(0)" ::: "memory");
  __builtin_amdgcn_s_barrier();
  asm volatile("" ::: "memory");
}
__device__ __forceinline__ void lds_fence() { asm volatile("s_waitcnt lgkmcnt(0)" ::: "memory"); }
#define PIN() do { asm volatile("" ::: "memory"); __builtin_amdgcn_sched_barrier(0); } while (0)

#define STAMP(k) do { } while (0)

struct Lds {
  float qpart[4][64];
  unsigned qs[4][64];
  float wmax[4][4];
  float wsum[4][4];
  float opart[4][512];
  float wgt[8][32];
  float wpart[8][64];
  float hs[64];
  float hstat[8][2];
  float upart[8][32];
  float fpart[8][64];
  unsigned tag[4];
  unsigned qcnt[4];
  float pad[19 * 1024];
};

__global__ void __launch_bounds__(NTHR, 2)
fused_decode_kernel(const unsigned short* __restrict__ x, const unsigned short* __restrict__ kc,
                    const unsigned short* __restrict__ vc, const unsigned short* __restrict__ wq,
                    const unsigned short* __restrict__ wo, const unsigned short* __restrict__ w1,
                    const unsigned short* __restrict__ b1, const unsigned short* __restrict__ w2,
                    const unsigned short* __restrict__ b2, unsigned short* __restrict__ out,
                    unsigned char* ws) {
  __shared__ __attribute__((aligned(16))) Lds L;
  unsigned cp0;
  {
    u64 pc0;
    asm volatile("s_getpc_b64 %0" : "=s"(pc0));
    typedef const __attribute__((address_space(1))) unsigned* gptr;
    const unsigned o0 = threadIdx.x * 128u;
    cp0 = *(gptr)((pc0 & ~(u64)255) + (o0 < CODE_BYTES ? o0 : 0u));
  }
  asm volatile("" :: "s"(x), "s"(kc), "s"(vc), "s"(wq), "s"(wo), "s"(w1), "s"(b1), "s"(w2), "s"(b2), "s"(out), "s"(ws));
  const int tid = threadIdx.x, lane = tid & 63, l15 = lane & 15, lq = lane >> 4;
  const int wave = __builtin_amdgcn_readfirstlane(tid >> 6);
  const int b = blockIdx.x, g = b & 7, i = b >> 3;
  u64* const h1 = (u64*)(ws + WS_H1);
  u64* const h2o = (u64*)(ws + WS_H2O);
  u64* const h3 = (u64*)(ws + WS_H3);
  u64* const h4 = (u64*)(ws + WS_H4);
  const __amdgpu_buffer_rsrc_t wsr = __builtin_amdgcn_make_buffer_rsrc(ws, 0, (int)WS_END, 0x00020000);
  const f32x4 zero4 = {0.f, 0.f, 0.f, 0.f};
  unsigned tag;
  const int rb = 2 * g + ((i >> 3) & 1), cb = i & 7;
  const int mb_ = (i - 16) * 8 + g;
  unsigned w1v[16];
  u32x2 w2v[8];
  STAMP(0);

  if (__builtin_expect(wave < 4, 1)) {
    const int a = wave, kr = i >> 3, cq = i & 7;
    const u32x4 xv = *reinterpret_cast<const u32x4*>(x + lane * 8);
    const unsigned ep = __hip_atomic_load(reinterpret_cast<const unsigned*>(ws + WS_EPOCH), __ATOMIC_RELAXED, __HIP_MEMORY_SCOPE_AGENT);
    u32x2 wqv[8];
    {
      const __amdgpu_buffer_rsrc_t wqr = __builtin_amdgcn_make_buffer_rsrc(const_cast<unsigned short*>(wq), 0, DM * HQ * DH * 2, 0x00020000);
      const int vo = (lq * 8 * (HQ * DH) + l15 * 4) * 2, so = ((kr * 128 + a * 32) * (HQ * DH) + g * 512 + cq * 64) * 2;
#pragma unroll
      for (int j = 0; j < 8; ++j) wqv[j] = __builtin_amdgcn_raw_buffer_load_b64(wqr, vo, so + j * (HQ * DH * 2), 2);
    }
    if (tid == 0) L.qcnt[0] = 0u;
    PIN();
    bar();
    PIN();
    float xf[8];
#pragma unroll
    for (int k = 0; k < 4; ++k) { xf[2 * k] = bflo(xv[k]); xf[2 * k + 1] = bfhi(xv[k]); }
    float s1 = 0.f;
#pragma unroll
    for (int k = 0; k < 8; ++k) s1 += xf[k];
    const float mean = wave_sum(s1) * (1.f / DM);
    float s2 = 0.f;
#pragma unroll
    for (int k = 0; k < 8; ++k) { const float d = xf[k] - mean; s2 += d * d; }
    const float var = wave_sum(s2) * (1.f / DM);
    const float mb = bf16r(mean), rden = __builtin_amdgcn_rcpf(bf16r(__builtin_amdgcn_sqrtf(bf16r(bf16r(var) + 1e-5f))));
    u32x4 xn;
#pragma unroll
    for (int k = 0; k < 4; ++k) xn[k] = pk2(bf16r(xf[2 * k] - mb) * rden, bf16r(xf[2 * k + 1] - mb) * rden);
    u32x4 xa;
    {
      const int src = (kr * 16 + a * 4 + lq) * 4;
#pragma unroll
      for (int k = 0; k < 4; ++k) xa[k] = (unsigned)__builtin_amdgcn_ds_bpermute(src, (int)xn[k]);
    }
    tag = ep + 1u;
    if (tid == 0) L.tag[0] = tag;
    {
      f32x4 qc[4];
      u32x4 f0, f1, f2, f3;
#pragma unroll
      for (int k = 0; k < 4; ++k) {
        f0[k] = plo(wqv[2 * k][0], wqv[2 * k + 1][0]); f1[k] = phi(wqv[2 * k][0], wqv[2 * k + 1][0]);
        f2[k] = plo(wqv[2 * k][1], wqv[2 * k + 1][1]); f3[k] = phi(wqv[2 * k][1], wqv[2 * k + 1][1]);
      }
      qc[0] = mfma(xa, f0, zero4); qc[1] = mfma(xa, f1, zero4); qc[2] = mfma(xa, f2, zero4); qc[3] = mfma(xa, f3, zero4);
      if (lane < 16) *reinterpret_cast<f32x4*>(&L.qpart[a][lane * 4]) = f32x4{qc[0][0], qc[1][0], qc[2][0], qc[3][0]};
    }
    STAMP(1);
    lds_fence();
    if (a != 0) {
      if (lane == 0) __hip_atomic_fetch_add(&L.qcnt[0], 1u, __ATOMIC_RELAXED, __HIP_MEMORY_SCOPE_WORKGROUP);
    } else {
      for (unsigned spins = 0; spins < SPIN_CAP; ++spins) {
        if (__hip_atomic_load(&L.qcnt[0], __ATOMIC_RELAXED, __HIP_MEMORY_SCOPE_WORKGROUP) == 3u) break;
      }
      asm volatile("" ::: "memory");
      const float qv = (L.qpart[0][lane] + L.qpart[1][lane]) + (L.qpart[2][lane] + L.qpart[3][lane]);
      put(h1 + g * (H1STRIDE / 8) + kr * 512 + cq * 64 + lane, qv, tag);
    }
    PIN();
    bar();
    PIN();
    STAMP(2);
    {
      const unsigned src = (unsigned)WS_H1 + g * H1STRIDE + (a * 128 + lane * 2) * 8;
      u32x4 v[4];
      {
        const u64 t0 = __builtin_amdgcn_s_memrealtime();
        while (__builtin_amdgcn_s_memrealtime() - t0 < QDELAY) __builtin_amdgcn_s_sleep(1);
      }
      PIN();
#pragma unroll
      for (int k = 0; k < 4; ++k) v[k] = get2(wsr, src + k * 4096);
      PIN();
      bar();
      PIN();
      for (unsigned spins = 0; spins < SPIN_CAP; ++spins) {
        if (gall2(v, tag)) break;
#pragma unroll
        for (int k = 0; k < 4; ++k) v[k] = get2(wsr, src + k * 4096);
      }
      const float q0 = (__uint_as_float(v[0][0]) + __uint_as_float(v[1][0])) + (__uint_as_float(v[2][0]) + __uint_as_float(v[3][0]));
      const float q1 = (__uint_as_float(v[0][2]) + __uint_as_float(v[1][2])) + (__uint_as_float(v[2][2]) + __uint_as_float(v[3][2]));
      L.qs[a][lane] = pk2(q0, q1);
    }
    asm volatile("" :: "v"(cp0));
    STAMP(3);
    bar();
    bar();
    bar();
  } else {
    const int w4 = wave - 4;
    const size_t kvbase = ((size_t)g * SK + (SK - WIN) + i * 128 + w4 * 32) * DH;
    bar();
    PIN();
    u32x4 kf[2][4];
    u32x4 vv[8];
#pragma unroll
    for (int c = 0; c < 4; ++c) kf[0][c] = ldw<u32x4>(kc + kvbase + (size_t)l15 * DH + c * 32 + lq * 8);
    PIN();
    bar();
    PIN();
#pragma unroll
    for (int c = 0; c < 4; ++c) kf[1][c] = ldw<u32x4>(kc + kvbase + (size_t)(16 + l15) * DH + c * 32 + lq * 8);
#pragma unroll
    for (int j = 0; j < VEARLY; ++j) vv[j] = ldw<u32x4>(vc + kvbase + (size_t)((j >> 2) * 16 + lq * 4 + (j & 3)) * DH + l15 * 8);
    PIN();
    bar();
    PIN();
#pragma unroll
    for (int j = VEARLY; j < 8; ++j) vv[j] = ldw<u32x4>(vc + kvbase + (size_t)((j >> 2) * 16 + lq * 4 + (j & 3)) * DH + l15 * 8);
    PIN();
    tag = L.tag[0];
    bar();
    asm volatile("" :: "v"(cp0));
    STAMP(1);
    f32x4 s0 = zero4, s1 = zero4;
    {
      const unsigned* qsrc = &L.qs[l15 & 3][lq * 4];
      u32x4 qa[4];
#pragma unroll
      for (int c = 0; c < 4; ++c) qa[c] = *reinterpret_cast<const u32x4*>(qsrc + c * 16);
#pragma unroll
      for (int c = 0; c < 4; ++c) { s0 = mfma(kf[0][c], qa[c], s0); s1 = mfma(kf[1][c], qa[c], s1); }
    }
    float sc[8];
#pragma unroll
    for (int r = 0; r < 4; r += 2) {
      unsigned t = pk2(s0[r], s0[r + 1]);
      t = pk2(bflo(t) * 0.08838834764831845f, bfhi(t) * 0.08838834764831845f);
      sc[r] = bflo(t); sc[r + 1] = bfhi(t);
      t = pk2(s1[r], s1[r + 1]);
      t = pk2(bflo(t) * 0.08838834764831845f, bfhi(t) * 0.08838834764831845f);
      sc[4 + r] = bflo(t); sc[5 + r] = bfhi(t);
    }
    {
      float mx = fmaxf(fmaxf(fmaxf(sc[0], sc[1]), fmaxf(sc[2], sc[3])), fmaxf(fmaxf(sc[4], sc[5]), fmaxf(sc[6], sc[7])));
      mx = sw32_max(sw16_max(mx));
      if (lane < 4) L.wmax[lane][w4] = mx;
    }
    STAMP(2);
    bar();
    u32x4 pb;
    {
      const f32x4 m4 = *reinterpret_cast<const f32x4*>(&L.wmax[l15 & 3][0]);
      const float M = fmaxf(fmaxf(m4[0], m4[1]), fmaxf(m4[2], m4[3]));
      float pp[8];
#pragma unroll
      for (int r = 0; r < 8; ++r) pp[r] = __expf(sc[r] - M);
      float ls = ((pp[0] + pp[1]) + (pp[2] + pp[3])) + ((pp[4] + pp[5]) + (pp[6] + pp[7]));
      ls = sw32_sum(sw16_sum(ls));
      if (lane < 4) L.wsum[lane][w4] = ls;
#pragma unroll
      for (int r = 0; r < 4; ++r) pb[r] = pk2(pp[2 * r], pp[2 * r + 1]);
    }
    f32x4 oc[8];
#pragma unroll
    for (int c2 = 0; c2 < 4; ++c2) {
      u32x4 f0, f1;
#pragma unroll
      for (int k = 0; k < 4; ++k) { f0[k] = plo(vv[2 * k][c2], vv[2 * k + 1][c2]); f1[k] = phi(vv[2 * k][c2], vv[2 * k + 1][c2]); }
      oc[2 * c2] = mfma(f0, pb, zero4);
      oc[2 * c2 + 1] = mfma(f1, pb, zero4);
    }
    if (l15 < 4) {
#pragma unroll
      for (int r = 0; r < 4; ++r) {
        float* dst = &L.opart[w4][l15 * 128 + (lq * 4 + r) * 8];
        *reinterpret_cast<f32x4*>(dst) = f32x4{oc[0][r], oc[1][r], oc[2][r], oc[3][r]};
        *reinterpret_cast<f32x4*>(dst + 4) = f32x4{oc[4][r], oc[5][r], oc[6][r], oc[7][r]};
      }
    }
    STAMP(3);
    bar();
  }

  {
    const float o = (L.opart[0][tid] + L.opart[1][tid]) + (L.opart[2][tid] + L.opart[3][tid]);
    put(h2o + ((size_t)g * 32 + i) * H2B + tid, o, tag);
    if (tid < 4) {
      const float l = (L.wsum[tid][0] + L.wsum[tid][1]) + (L.wsum[tid][2] + L.wsum[tid][3]);
      const float m = fmaxf(fmaxf(L.wmax[tid][0], L.wmax[tid][1]), fmaxf(L.wmax[tid][2], L.wmax[tid][3]));
      put(h2o + ((size_t)g * 32 + i) * H2B + 512 + tid * 2, m, tag);
      put(h2o + ((size_t)g * 32 + i) * H2B + 513 + tid * 2, l, tag);
    }
  }
  STAMP(4);
  if (i < 16) {
    PIN();
    u32x2 wov[8];
#pragma unroll
    for (int j = 0; j < 8; ++j)
      wov[j] = ldw<u32x2>(wo + (size_t)(rb * 256 + wave * 32 + lq * 8 + j) * DM + cb * 64 + l15 * 4);
    PIN();
    const int hl = (rb & 1) * 2 + (wave >> 2);
    const unsigned so = (unsigned)WS_H2O + ((g * 32 + (l15 >> 2) * 8) * H2B + hl * 128 + (wave & 3) * 32 + lq * 8 + (l15 & 3) * 2) * 8;
    const unsigned ss = (unsigned)WS_H2O + ((g * 32 + (lane & 31)) * H2B + 512 + hl * 2) * 8;
    u32x4 v[9];
    {
      const u64 t0 = __builtin_amdgcn_s_memrealtime();
      while (__builtin_amdgcn_s_memrealtime() - t0 < H2MODE) __builtin_amdgcn_s_sleep(1);
    }
    PIN();
    v[8] = get2(wsr, ss);
#pragma unroll
    for (int k = 0; k < 8; ++k) v[k] = get2(wsr, so + k * (H2B * 8));
    PIN();
    u32x4 f0, f1, f2, f3;
#pragma unroll
    for (int k = 0; k < 4; ++k) {
      f0[k] = plo(wov[2 * k][0], wov[2 * k + 1][0]); f1[k] = phi(wov[2 * k][0], wov[2 * k + 1][0]);
      f2[k] = plo(wov[2 * k][1], wov[2 * k + 1][1]); f3[k] = phi(wov[2 * k][1], wov[2 * k + 1][1]);
    }
    asm volatile("" : "+v"(f0), "+v"(f1), "+v"(f2), "+v"(f3));
    if (!gall2(v, tag)) {
      for (unsigned spins = 0; spins < SPIN_CAP; ++spins) {
        bool all = true;
        if (!gok2(v[8], tag)) { all = false; v[8] = get2(wsr, ss); }
#pragma unroll
        for (int k = 0; k < 8; ++k) { if (!gok2(v[k], tag)) { all = false; v[k] = get2(wsr, so + k * (H2B * 8)); } }
        if (all) break;
      }
    }
    STAMP(5);
    const float mm = __uint_as_float(v[8][0]);
    float M = sw16_max(row16_max(mm));
    const float wg = __expf(mm - M);
    if (lane < 32) L.wgt[wave][lane] = wg;
    const float Ls = sw16_sum(row16_sum(wg * __uint_as_float(v[8][2])));
    lds_fence();
    float o0 = 0.f, o1 = 0.f;
#pragma unroll
    for (int k4 = 0; k4 < 2; ++k4) {
      const f32x4 wv = *reinterpret_cast<const f32x4*>(&L.wgt[wave][(l15 >> 2) * 8 + k4 * 4]);
#pragma unroll
      for (int k = 0; k < 4; ++k) { o0 = fmaf(wv[k], __uint_as_float(v[k4 * 4 + k][0]), o0); o1 = fmaf(wv[k], __uint_as_float(v[k4 * 4 + k][2]), o1); }
    }
    o0 += dpp<ROR4>(o0); o0 += dpp<ROR8>(o0); o1 += dpp<ROR4>(o1); o1 += dpp<ROR8>(o1);
    const float rl = __builtin_amdgcn_rcpf(Ls);
    const u32x4 oa = quad_gather(pk2(o0 * rl, o1 * rl));
    const f32x4 c0 = mfma(oa, f0, zero4), c1 = mfma(oa, f1, zero4), c2 = mfma(oa, f2, zero4), c3 = mfma(oa, f3, zero4);
    if (lane < 16) *reinterpret_cast<f32x4*>(&L.wpart[wave][lane * 4]) = f32x4{c0[0], c1[0], c2[0], c3[0]};
    bar();
    if (tid < 64) {
      float t = 0.f;
#pragma unroll
      for (int k = 0; k < 8; ++k) t += L.wpart[k][tid];
      put(h3 + (size_t)rb * 512 + cb * 64 + tid, t, tag);
    }
    STAMP(6);
    return;
  }
  if (mb_ >= NMLP + NFIN) return;
  if (mb_ >= NMLP) {
    const int fb = mb_ - NMLP;
    const unsigned xr = *reinterpret_cast<const unsigned*>(x + fb * 64 + (lane & 31) * 2);
    const unsigned short b2r = b2[fb * 64 + lane];
    if (wave == 0) {
      const unsigned src = (unsigned)WS_H3 + ((lane >> 5) * 8 * 512 + fb * 64 + (lane & 31) * 2) * 8;
      u32x4 v[8];
      for (unsigned spins = 0; spins < SPIN_CAP; ++spins) {
#pragma unroll
        for (int k = 0; k < 8; ++k) v[k] = get2(wsr, src + k * 4096);
        if (gall2(v, tag)) break;
        __builtin_amdgcn_s_sleep(1);
      }
      float t0 = 0.f, t1 = 0.f;
#pragma unroll
      for (int k = 0; k < 8; ++k) { t0 += __uint_as_float(v[k][0]); t1 += __uint_as_float(v[k][2]); }
      t0 = sw32_sum(t0); t1 = sw32_sum(t1);
      if (lane < 32) *reinterpret_cast<f32x2*>(&L.hs[lane * 2]) = f32x2{bf16r(bflo(xr) + bf16r(t0)), bf16r(bfhi(xr) + bf16r(t1))};
    }
    {
      u32x4 v[4];
      const int p0 = wave * 7 + (lane >> 5) * 4;
      const unsigned base = (unsigned)WS_H4 + (p0 * 512 + fb * 64 + (lane & 31) * 2) * 8;
      const unsigned last = (lane >> 5) ? 2u * 4096u : 3u * 4096u;
      for (unsigned spins = 0; spins < SPIN_CAP; ++spins) {
#pragma unroll
        for (int k = 0; k < 3; ++k) v[k] = get2(wsr, base + k * 4096);
        v[3] = get2(wsr, base + last);
        if (gall2(v, tag)) break;
        __builtin_amdgcn_s_sleep(1);
      }
      const float w3 = (lane >> 5) ? 0.f : 1.f;
      float t0 = (__uint_as_float(v[0][0]) + __uint_as_float(v[1][0])) + (__uint_as_float(v[2][0]) + w3 * __uint_as_float(v[3][0]));
      float t1 = (__uint_as_float(v[0][2]) + __uint_as_float(v[1][2])) + (__uint_as_float(v[2][2]) + w3 * __uint_as_float(v[3][2]));
      t0 = sw32_sum(t0); t1 = sw32_sum(t1);
      if (lane < 32) *reinterpret_cast<f32x2*>(&L.fpart[wave][lane * 2]) = f32x2{t0, t1};
    }
    STAMP(9);
    bar();
    if (tid < 64) {
      const int slot = (tid & 3) * 16 + (tid >> 2);
      float t = 0.f;
#pragma unroll
      for (int k = 0; k < 8; ++k) t += L.fpart[k][slot];
      const float y = bf16r(bf16r(t) + bflo(b2r));
      const float r = bf16r(L.hs[tid] + y);
      out[fb * 64 + tid] = (unsigned short)(__float_as_uint(r) >> 16);
      if (fb == 0 && tid == 0) *reinterpret_cast<unsigned*>(ws + WS_EPOCH) = tag;
    }
    STAMP(10);
    return;
  }

  PIN();
  {
#pragma unroll
    for (int j = 0; j < 16; ++j)
      w1v[j] = ldw<unsigned>(w1 + (size_t)(wave * 64 + (j >> 3) * 32 + lq * 8 + (j & 7)) * FF + mb_ * 32 + l15 * 2);
#pragma unroll
    for (int j = 0; j < 8; ++j)
      w2v[j] = ldw<u32x2>(w2 + (size_t)(mb_ * 32 + lq * 8 + j) * DM + wave * 64 + l15 * 4);
  }
  const int cp = ((l15 >> 2) & 1) * 16 + lq * 4 + (l15 & 3);
  const unsigned xr = *reinterpret_cast<const unsigned*>(x + wave * 64 + cp * 2);
  const unsigned b1r = *reinterpret_cast<const unsigned*>(b1 + mb_ * 32 + lq * 8 + (l15 & 3) * 2);
  PIN();
  u32x4 e0[2], e1[2], f0, f1, f2, f3;
#pragma unroll
  for (int ks = 0; ks < 2; ++ks)
#pragma unroll
    for (int k = 0; k < 4; ++k) { e0[ks][k] = plo(w1v[ks * 8 + 2 * k], w1v[ks * 8 + 2 * k + 1]); e1[ks][k] = phi(w1v[ks * 8 + 2 * k], w1v[ks * 8 + 2 * k + 1]); }
#pragma unroll
  for (int k = 0; k < 4; ++k) {
    f0[k] = plo(w2v[2 * k][0], w2v[2 * k + 1][0]); f1[k] = phi(w2v[2 * k][0], w2v[2 * k + 1][0]);
    f2[k] = plo(w2v[2 * k][1], w2v[2 * k + 1][1]); f3[k] = phi(w2v[2 * k][1], w2v[2 * k + 1][1]);
  }
  PIN();
  asm volatile("" : "+v"(e0[0]), "+v"(e0[1]), "+v"(e1[0]), "+v"(e1[1]), "+v"(f0), "+v"(f1), "+v"(f2), "+v"(f3));
  float hv0, hv1;
  {
    const unsigned src = (unsigned)WS_H3 + ((l15 >> 3) * 8 * 512 + wave * 64 + cp * 2) * 8;
    u32x4 v[8];
    for (unsigned spins = 0; spins < SPIN_CAP; ++spins) {
#pragma unroll
      for (int k = 0; k < 8; ++k) v[k] = get2(wsr, src + k * 4096);
      if (gall2(v, tag)) break;
      __builtin_amdgcn_s_sleep(1);
    }
    float t0 = 0.f, t1 = 0.f;
#pragma unroll
    for (int k = 0; k < 8; ++k) { t0 += __uint_as_float(v[k][0]); t1 += __uint_as_float(v[k][2]); }
    t0 += dpp<ROR8>(t0); t1 += dpp<ROR8>(t1);
    hv0 = bf16r(bflo(xr) + bf16r(t0));
    hv1 = bf16r(bfhi(xr) + bf16r(t1));
    const float a1 = wave_sum(hv0 + hv1), a2 = wave_sum(hv0 * hv0 + hv1 * hv1);
    if (lane == 0) *reinterpret_cast<f32x2*>(&L.hstat[wave][0]) = f32x2{a1, a2};
  }
  STAMP(7);
  bar();
  {
    float t1 = 0.f, t2 = 0.f;
#pragma unroll
    for (int k = 0; k < 4; ++k) { const f32x4 t = *reinterpret_cast<const f32x4*>(&L.hstat[2 * k][0]); t1 += t[0] + t[2]; t2 += t[1] + t[3]; }
    const float mean2 = t1 * (0.5f / DM);
    const float var2 = fmaxf(t2 * (0.5f / DM) - mean2 * mean2, 0.f);
    const float mb2 = bf16r(mean2), rden2 = __builtin_amdgcn_rcpf(bf16r(__builtin_amdgcn_sqrtf(bf16r(bf16r(var2) + 1e-5f))));
    const u32x4 ha = quad_gather(pk2(bf16r(hv0 - mb2) * rden2, bf16r(hv1 - mb2) * rden2));
    const f32x4 c0a = mfma(ha, e0[0], zero4), c1a = mfma(ha, e1[0], zero4), c0b = mfma(ha, e0[1], zero4), c1b = mfma(ha, e1[1], zero4);
    const float u0 = sw16_sum(lane < 16 ? c0a[0] : c0b[0]), u1 = sw16_sum(lane < 16 ? c1a[0] : c1b[0]);
    if (lane < 16) *reinterpret_cast<f32x2*>(&L.upart[wave][lane * 2]) = f32x2{u0, u1};
  }
  bar();
  {
    float u0 = 0.f, u1 = 0.f;
#pragma unroll
    for (int k = 0; k < 8; ++k) { const f32x2 t = *reinterpret_cast<const f32x2*>(&L.upart[k][lq * 8 + (l15 & 3) * 2]); u0 += t[0]; u1 += t[1]; }
    u0 = bf16r(bf16r(u0) + bflo(b1r)); u1 = bf16r(bf16r(u1) + bfhi(b1r));
    const float z0 = 0.7978845608028654f * (u0 + 0.044715f * u0 * u0 * u0), z1 = 0.7978845608028654f * (u1 + 0.044715f * u1 * u1 * u1);
    const float th0 = 1.f - 2.f * __builtin_amdgcn_rcpf(__expf(2.f * z0) + 1.f), th1 = 1.f - 2.f * __builtin_amdgcn_rcpf(__expf(2.f * z1) + 1.f);
    const u32x4 ga = quad_gather(pk2(0.5f * u0 * (1.f + th0), 0.5f * u1 * (1.f + th1)));
    const f32x4 c0 = mfma(ga, f0, zero4), c1 = mfma(ga, f1, zero4), c2 = mfma(ga, f2, zero4), c3 = mfma(ga, f3, zero4);
    if (lane < 16) {
      u64* dst = h4 + (size_t)mb_ * 512 + wave * 64 + lane;
      put(dst, c0[0], tag); put(dst + 16, c1[0], tag); put(dst + 32, c2[0], tag); put(dst + 48, c3[0], tag);
    }
  }
  STAMP(8);
}

extern "C" void kernel_launch(void* const* d_in, const int* in_sizes, int n_in,
                              void* d_out, int out_size, void* d_ws, size_t ws_size,
                              hipStream_t stream) {
  const unsigned short* x = (const unsigned short*)d_in[0];
  const unsigned short* kc = (const unsigned short*)d_in[1];
  const unsigned short* vc = (const unsigned short*)d_in[2];
  const unsigned short* wq = (const unsigned short*)d_in[3];
  const unsigned short* wo = (const unsigned short*)d_in[4];
  const unsigned short* w1 = (const unsigned short*)d_in[6];
  const unsigned short* b1 = (const unsigned short*)d_in[7];
  const unsigned short* w2 = (const unsigned short*)d_in[8];
  const unsigned short* b2 = (const unsigned short*)d_in[9];
  fused_decode_kernel<<<NBLK, NTHR, 0, stream>>>(x, kc, vc, wq, wo, w1, b1, w2, b2,
                                                 (unsigned short*)d_out, (unsigned char*)d_ws);
}
```

```cpp
#include <hip/hip_runtime.h>
#include <hip/hip_bf16.h>
#include <stdint.h>

typedef unsigned long long u64;
typedef __attribute__((ext_vector_type(4))) unsigned u32x4;
typedef __attribute__((ext_vector_type(2))) unsigned u32x2;
typedef __attribute__((ext_vector_type(4))) float f32x4;
typedef __attribute__((ext_vector_type(2))) float f32x2;
typedef __attribute__((ext_vector_type(8))) short s16x8;

constexpr int DM = 512, HQ = 32, HKV = 8, SK = 65536, DH = 128, WIN = 4096, FF = 1792;
constexpr int NBLK = 256, NTHR = 512;
constexpr int NMLP = 56, NFIN = 8;

constexpr size_t WS_EPOCH = 0;
constexpr unsigned H1STRIDE = 128 * 1024;
constexpr size_t WS_H1 = 4096;
constexpr size_t WS_H2O = WS_H1 + (size_t)8 * H1STRIDE;
constexpr int H2B = 528;
constexpr size_t WS_H3 = WS_H2O + (size_t)8 * 32 * H2B * 8;
constexpr size_t WS_H4 = WS_H3 + (size_t)16 * 512 * 8;
constexpr size_t WS_END = WS_H4 + (size_t)NMLP * 512 * 8;

constexpr unsigned SPIN_CAP = 1u << 18;
#define QDELAY 100
#define H2MODE 110
constexpr unsigned CODE_BYTES = 11264;
#define VEARLY 3

__device__ __forceinline__ float bf16r(float x) { return __bfloat162float(__float2bfloat16(x)); }
__device__ __forceinline__ float bflo(unsigned u) { return __uint_as_float(u << 16); }
__device__ __forceinline__ float bfhi(unsigned u) { return __uint_as_float(u & 0xffff0000u); }
__device__ __forceinline__ unsigned pk2(float lo, float hi) {
  return (__float_as_uint(bf16r(lo)) >> 16) | (__float_as_uint(bf16r(hi)) & 0xffff0000u);
}
__device__ __forceinline__ unsigned plo(unsigned a, unsigned b) { return __builtin_amdgcn_perm(b, a, 0x05040100u); }
__device__ __forceinline__ unsigned phi(unsigned a, unsigned b) { return __builtin_amdgcn_perm(b, a, 0x07060302u); }

template <int CTRL> __device__ __forceinline__ float dpp(float x) {
  return __builtin_bit_cast(float, __builtin_amdgcn_mov_dpp(__builtin_bit_cast(int, x), CTRL, 0xf, 0xf, true));
}
constexpr int XOR1 = 0xB1, XOR2 = 0x4E, ROR4 = 0x124, ROR8 = 0x128;
__device__ __forceinline__ float sw16_sum(float x) {
  auto s = __builtin_amdgcn_permlane16_swap(__float_as_uint(x), __float_as_uint(x), false, false);
  return __uint_as_float(s[0]) + __uint_as_float(s[1]);
}
__device__ __forceinline__ float sw32_sum(float x) {
  auto s = __builtin_amdgcn_permlane32_swap(__float_as_uint(x), __float_as_uint(x), false, false);
  return __uint_as_float(s[0]) + __uint_as_float(s[1]);
}
__device__ __forceinline__ float sw16_max(float x) {
  auto s = __builtin_amdgcn_permlane16_swap(__float_as_uint(x), __float_as_uint(x), false, false);
  return fmaxf(__uint_as_float(s[0]), __uint_as_float(s[1]));
}
__device__ __forceinline__ float sw32_max(float x) {
  auto s = __builtin_amdgcn_permlane32_swap(__float_as_uint(x), __float_as_uint(x), false, false);
  return fmaxf(__uint_as_float(s[0]), __uint_as_float(s[1]));
}
__device__ __forceinline__ float row16_sum(float x) {
  x += dpp<XOR1>(x); x += dpp<XOR2>(x); x += dpp<ROR4>(x); x += dpp<ROR8>(x); return x;
}
__device__ __forceinline__ float row16_max(float x) {
  x = fmaxf(x, dpp<XOR1>(x)); x = fmaxf(x, dpp<XOR2>(x)); x = fmaxf(x, dpp<ROR4>(x)); x = fmaxf(x, dpp<ROR8>(x)); return x;
}
template <int CTRL> __device__ __forceinline__ unsigned dppu(unsigned x) {
  return (unsigned)__builtin_amdgcn_mov_dpp((int)x, CTRL, 0xf, 0xf, true);
}
__device__ __forceinline__ u32x4 quad_gather(unsigned x) {
  return u32x4{dppu<0x00>(x), dppu<0x55>(x), dppu<0xAA>(x), dppu<0xFF>(x)};
}
__device__ __forceinline__ float wave_sum(float x) { return sw32_sum(sw16_sum(row16_sum(x))); }
__device__ __forceinline__ f32x4 mfma(u32x4 a, u32x4 b, f32x4 c) {
  return __builtin_amdgcn_mfma_f32_16x16x32_bf16(__builtin_bit_cast(s16x8, a), __builtin_bit_cast(s16x8, b), c, 0, 0, 0);
}

template <typename T> __device__ __forceinline__ T ldw(const void* p) {
  return __builtin_nontemporal_load(reinterpret_cast<const T*>(p));
}

__device__ __forceinline__ void put(u64* g, float v, unsigned tag) {
  __hip_atomic_store(g, ((u64)tag << 32) | (__float_as_uint(v) & 0xffffff00u), __ATOMIC_RELAXED, __HIP_MEMORY_SCOPE_AGENT);
}
__device__ __forceinline__ u32x4 get2(__amdgpu_buffer_rsrc_t r, unsigned off) {
  asm volatile("" ::: "memory");
  return __builtin_amdgcn_raw_buffer_load_b128(r, (int)off, 0, 16);
}
template <int N> __device__ __forceinline__ bool gall2(const u32x4 (&v)[N], unsigned tag) {
  bool ok = true; unsigned lo = 0;
#pragma unroll
  for (int k = 0; k < N; ++k) { ok &= (v[k][1] == tag) & (v[k][3] == tag); lo |= v[k][0] | v[k][2]; }
  ok &= (lo & 0xffu) == 0u;
  return __all(ok);
}
__device__ __forceinline__ bool gok2(u32x4 v, unsigned tag) {
  return __all((v[1] == tag) & (v[3] == tag) & (((v[0] | v[2]) & 0xffu) == 0u));
}
__device__ __forceinline__ void bar() {
  asm volatile("s_waitcnt lgkmcnt(0)" ::: "memory");
  __builtin_amdgcn_s_barrier();
  asm volatile("" ::: "memory");
}
__device__ __forceinline__ void lds_fence() { asm volatile("s_waitcnt lgkmcnt(0)" ::: "memory"); }
#define PIN() do { asm volatile("" ::: "memory"); __builtin_amdgcn_sched_barrier(0); } while (0)

#define STAMP(k) do { } while (0)

struct Lds {
  float qpart[4][64];
  unsigned qs[4][64];
  float wmax[4][4];
  float wsum[4][4];
  float opart[4][512];
  float wgt[8][32];
  float wpart[8][64];
  float hs[64];
  float hstat[8][2];
  float upart[8][32];
  float fpart[8][64];
  unsigned tag[4];
  unsigned qcnt[4];
  float pad[19 * 1024];
};

__global__ void __launch_bounds__(NTHR, 2)
fused_decode_kernel(const unsigned short* __restrict__ x, const unsigned short* __restrict__ kc,
                    const unsigned short* __restrict__ vc, const unsigned short* __restrict__ wq,
                    unsigned char* ws, const unsigned short* __restrict__ wo,
                    const unsigned short* __restrict__ w1, const unsigned short* __restrict__ w2,
                    const unsigned short* __restrict__ b1, const unsigned short* __restrict__ b2,
                    unsigned short* __restrict__ out) {
  __shared__ __attribute__((aligned(16))) Lds L;
  unsigned cp0;
  {
    u64 pc0;
    asm volatile("s_getpc_b64 %0" : "=s"(pc0));
    typedef const __attribute__((address_space(1))) unsigned* gptr;
    const unsigned o0 = threadIdx.x * 128u;
    cp0 = *(gptr)((pc0 & ~(u64)255) + (o0 < CODE_BYTES ? o0 : 0u));
  }
  asm volatile("" :: "s"(x), "s"(kc), "s"(vc), "s"(wq), "s"(ws), "s"(wo), "s"(w1), "s"(w2));
  const int tid = threadIdx.x, lane = tid & 63, l15 = lane & 15, lq = lane >> 4;
  const int wave = __builtin_amdgcn_readfirstlane(tid >> 6);
  const int b = blockIdx.x, g = b & 7, i = b >> 3;
  u64* const h1 = (u64*)(ws + WS_H1);
  u64* const h2o = (u64*)(ws + WS_H2O);
  u64* const h3 = (u64*)(ws + WS_H3);
  u64* const h4 = (u64*)(ws + WS_H4);
  const __amdgpu_buffer_rsrc_t wsr = __builtin_amdgcn_make_buffer_rsrc(ws, 0, (int)WS_END, 0x00020000);
  const f32x4 zero4 = {0.f, 0.f, 0.f, 0.f};
  unsigned tag;
  const int rb = 2 * g + ((i >> 3) & 1), cb = i & 7;
  const int mb_ = (i - 16) * 8 + g;
  unsigned w1v[16];
  u32x2 w2v[8];
  STAMP(0);

  if (__builtin_expect(wave < 4, 1)) {
    const int a = wave, kr = i >> 3, cq = i & 7;
    const u32x4 xv = *reinterpret_cast<const u32x4*>(x + lane * 8);
    const unsigned ep = __hip_atomic_load(reinterpret_cast<const unsigned*>(ws + WS_EPOCH), __ATOMIC_RELAXED, __HIP_MEMORY_SCOPE_AGENT);
    u32x2 wqv[8];
    {
      const __amdgpu_buffer_rsrc_t wqr = __builtin_amdgcn_make_buffer_rsrc(const_cast<unsigned short*>(wq), 0, DM * HQ * DH * 2, 0x00020000);
      const int vo = (lq * 8 * (HQ * DH) + l15 * 4) * 2, so = ((kr * 128 + a * 32) * (HQ * DH) + g * 512 + cq * 64) * 2;
#pragma unroll
      for (int j = 0; j < 8; ++j) wqv[j] = __builtin_amdgcn_raw_buffer_load_b64(wqr, vo, so + j * (HQ * DH * 2), 2);
    }
    if (tid == 0) L.qcnt[0] = 0u;
    PIN();
    bar();
    PIN();
    float xf[8];
#pragma unroll
    for (int k = 0; k < 4; ++k) { xf[2 * k] = bflo(xv[k]); xf[2 * k + 1] = bfhi(xv[k]); }
    float s1 = 0.f;
#pragma unroll
    for (int k = 0; k < 8; ++k) s1 += xf[k];
    const float mean = wave_sum(s1) * (1.f / DM);
    float s2 = 0.f;
#pragma unroll
    for (int k = 0; k < 8; ++k) { const float d = xf[k] - mean; s2 += d * d; }
    const float var = wave_sum(s2) * (1.f / DM);
    const float mb = bf16r(mean), rden = __builtin_amdgcn_rcpf(bf16r(__builtin_amdgcn_sqrtf(bf16r(bf16r(var) + 1e-5f))));
    u32x4 xn;
#pragma unroll
    for (int k = 0; k < 4; ++k) xn[k] = pk2(bf16r(xf[2 * k] - mb) * rden, bf16r(xf[2 * k + 1] - mb) * rden);
    u32x4 xa;
    {
      const int src = (kr * 16 + a * 4 + lq) * 4;
#pragma unroll
      for (int k = 0; k < 4; ++k) xa[k] = (unsigned)__builtin_amdgcn_ds_bpermute(src, (int)xn[k]);
    }
    tag = ep + 1u;
    if (tid == 0) L.tag[0] = tag;
    {
      f32x4 qc[4];
      u32x4 f0, f1, f2, f3;
#pragma unroll
      for (int k = 0; k < 4; ++k) {
        f0[k] = plo(wqv[2 * k][0], wqv[2 * k + 1][0]); f1[k] = phi(wqv[2 * k][0], wqv[2 * k + 1][0]);
        f2[k] = plo(wqv[2 * k][1], wqv[2 * k + 1][1]); f3[k] = phi(wqv[2 * k][1], wqv[2 * k + 1][1]);
      }
      qc[0] = mfma(xa, f0, zero4); qc[1] = mfma(xa, f1, zero4); qc[2] = mfma(xa, f2, zero4); qc[3] = mfma(xa, f3, zero4);
      if (lane < 16) *reinterpret_cast<f32x4*>(&L.qpart[a][lane * 4]) = f32x4{qc[0][0], qc[1][0], qc[2][0], qc[3][0]};
    }
    STAMP(1);
    lds_fence();
    if (a != 0) {
      if (lane == 0) __hip_atomic_fetch_add(&L.qcnt[0], 1u, __ATOMIC_RELAXED, __HIP_MEMORY_SCOPE_WORKGROUP);
    } else {
      for (unsigned spins = 0; spins < SPIN_CAP; ++spins) {
        if (__hip_atomic_load(&L.qcnt[0], __ATOMIC_RELAXED, __HIP_MEMORY_SCOPE_WORKGROUP) == 3u) break;
      }
      asm volatile("" ::: "memory");
      const float qv = (L.qpart[0][lane] + L.qpart[1][lane]) + (L.qpart[2][lane] + L.qpart[3][lane]);
      put(h1 + g * (H1STRIDE / 8) + kr * 512 + cq * 64 + lane, qv, tag);
    }
    PIN();
    bar();
    PIN();
    STAMP(2);
    {
      const unsigned src = (unsigned)WS_H1 + g * H1STRIDE + (a * 128 + lane * 2) * 8;
      u32x4 v[4];
      {
        const u64 t0 = __builtin_amdgcn_s_memrealtime();
        while (__builtin_amdgcn_s_memrealtime() - t0 < QDELAY) __builtin_amdgcn_s_sleep(1);
      }
      PIN();
#pragma unroll
      for (int k = 0; k < 4; ++k) v[k] = get2(wsr, src + k * 4096);
      PIN();
      bar();
      PIN();
      for (unsigned spins = 0; spins < SPIN_CAP; ++spins) {
        if (gall2(v, tag)) break;
#pragma unroll
        for (int k = 0; k < 4; ++k) v[k] = get2(wsr, src + k * 4096);
      }
      const float q0 = (__uint_as_float(v[0][0]) + __uint_as_float(v[1][0])) + (__uint_as_float(v[2][0]) + __uint_as_float(v[3][0]));
      const float q1 = (__uint_as_float(v[0][2]) + __uint_as_float(v[1][2])) + (__uint_as_float(v[2][2]) + __uint_as_float(v[3][2]));
      L.qs[a][lane] = pk2(q0, q1);
    }
    asm volatile("" :: "v"(cp0));
    STAMP(3);
    bar();
    bar();
    bar();
  } else {
    const int w4 = wave - 4;
    const size_t kvbase = ((size_t)g * SK + (SK - WIN) + i * 128 + w4 * 32) * DH;
    bar();
    PIN();
    u32x4 kf[2][4];
    u32x4 vv[8];
#pragma unroll
    for (int c = 0; c < 4; ++c) kf[0][c] = ldw<u32x4>(kc + kvbase + (size_t)l15 * DH + c * 32 + lq * 8);
    PIN();
    bar();
    PIN();
#pragma unroll
    for (int c = 0; c < 4; ++c) kf[1][c] = ldw<u32x4>(kc + kvbase + (size_t)(16 + l15) * DH + c * 32 + lq * 8);
#pragma unroll
    for (int j = 0; j < VEARLY; ++j) vv[j] = ldw<u32x4>(vc + kvbase + (size_t)((j >> 2) * 16 + lq * 4 + (j & 3)) * DH + l15 * 8);
    PIN();
    bar();
    PIN();
#pragma unroll
    for (int j = VEARLY; j < 8; ++j) vv[j] = ldw<u32x4>(vc + kvbase + (size_t)((j >> 2) * 16 + lq * 4 + (j & 3)) * DH + l15 * 8);
    PIN();
    tag = L.tag[0];
    bar();
    asm volatile("" :: "v"(cp0));
    STAMP(1);
    f32x4 s0 = zero4, s1 = zero4;
    {
      const unsigned* qsrc = &L.qs[l15 & 3][lq * 4];
      u32x4 qa[4];
#pragma unroll
      for (int c = 0; c < 4; ++c) qa[c] = *reinterpret_cast<const u32x4*>(qsrc + c * 16);
#pragma unroll
      for (int c = 0; c < 4; ++c) { s0 = mfma(kf[0][c], qa[c], s0); s1 = mfma(kf[1][c], qa[c], s1); }
    }
    float sc[8];
#pragma unroll
    for (int r = 0; r < 4; r += 2) {
      unsigned t = pk2(s0[r], s0[r + 1]);
      t = pk2(bflo(t) * 0.08838834764831845f, bfhi(t) * 0.08838834764831845f);
      sc[r] = bflo(t); sc[r + 1] = bfhi(t);
      t = pk2(s1[r], s1[r + 1]);
      t = pk2(bflo(t) * 0.08838834764831845f, bfhi(t) * 0.08838834764831845f);
      sc[4 + r] = bflo(t); sc[5 + r] = bfhi(t);
    }
    {
      float mx = fmaxf(fmaxf(fmaxf(sc[0], sc[1]), fmaxf(sc[2], sc[3])), fmaxf(fmaxf(sc[4], sc[5]), fmaxf(sc[6], sc[7])));
      mx = sw32_max(sw16_max(mx));
      if (lane < 4) L.wmax[lane][w4] = mx;
    }
    STAMP(2);
    bar();
    u32x4 pb;
    {
      const f32x4 m4 = *reinterpret_cast<const f32x4*>(&L.wmax[l15 & 3][0]);
      const float M = fmaxf(fmaxf(m4[0], m4[1]), fmaxf(m4[2], m4[3]));
      float pp[8];
#pragma unroll
      for (int r = 0; r < 8; ++r) pp[r] = __expf(sc[r] - M);
      float ls = ((pp[0] + pp[1]) + (pp[2] + pp[3])) + ((pp[4] + pp[5]) + (pp[6] + pp[7]));
      ls = sw32_sum(sw16_sum(ls));
      if (lane < 4) L.wsum[lane][w4] = ls;
#pragma unroll
      for (int r = 0; r < 4; ++r) pb[r] = pk2(pp[2 * r], pp[2 * r + 1]);
    }
    f32x4 oc[8];
#pragma unroll
    for (int c2 = 0; c2 < 4; ++c2) {
      u32x4 f0, f1;
#pragma unroll
      for (int k = 0; k < 4; ++k) { f0[k] = plo(vv[2 * k][c2], vv[2 * k + 1][c2]); f1[k] = phi(vv[2 * k][c2], vv[2 * k + 1][c2]); }
      oc[2 * c2] = mfma(f0, pb, zero4);
      oc[2 * c2 + 1] = mfma(f1, pb, zero4);
    }
    if (l15 < 4) {
#pragma unroll
      for (int r = 0; r < 4; ++r) {
        float* dst = &L.opart[w4][l15 * 128 + (lq * 4 + r) * 8];
        *reinterpret_cast<f32x4*>(dst) = f32x4{oc[0][r], oc[1][r], oc[2][r], oc[3][r]};
        *reinterpret_cast<f32x4*>(dst + 4) = f32x4{oc[4][r], oc[5][r], oc[6][r], oc[7][r]};
      }
    }
    STAMP(3);
    bar();
  }

  {
    const float o = (L.opart[0][tid] + L.opart[1][tid]) + (L.opart[2][tid] + L.opart[3][tid]);
    put(h2o + ((size_t)g * 32 + i) * H2B + tid, o, tag);
    if (tid < 4) {
      const float l = (L.wsum[tid][0] + L.wsum[tid][1]) + (L.wsum[tid][2] + L.wsum[tid][3]);
      const float m = fmaxf(fmaxf(L.wmax[tid][0], L.wmax[tid][1]), fmaxf(L.wmax[tid][2], L.wmax[tid][3]));
      put(h2o + ((size_t)g * 32 + i) * H2B + 512 + tid * 2, m, tag);
      put(h2o + ((size_t)g * 32 + i) * H2B + 513 + tid * 2, l, tag);
    }
  }
  STAMP(4);
  if (i < 16) {
    PIN();
    u32x2 wov[8];
#pragma unroll
    for (int j = 0; j < 8; ++j)
      wov[j] = ldw<u32x2>(wo + (size_t)(rb * 256 + wave * 32 + lq * 8 + j) * DM + cb * 64 + l15 * 4);
    PIN();
    const int hl = (rb & 1) * 2 + (wave >> 2);
    const unsigned so = (unsigned)WS_H2O + ((g * 32 + (l15 >> 2) * 8) * H2B + hl * 128 + (wave & 3) * 32 + lq * 8 + (l15 & 3) * 2) * 8;
    const unsigned ss = (unsigned)WS_H2O + ((g * 32 + (lane & 31)) * H2B + 512 + hl * 2) * 8;
    u32x4 v[9];
    {
      const u64 t0 = __builtin_amdgcn_s_memrealtime();
      while (__builtin_amdgcn_s_memrealtime() - t0 < H2MODE) __builtin_amdgcn_s_sleep(1);
    }
    PIN();
    v[8] = get2(wsr, ss);
#pragma unroll
    for (int k = 0; k < 8; ++k) v[k] = get2(wsr, so + k * (H2B * 8));
    PIN();
    u32x4 f0, f1, f2, f3;
#pragma unroll
    for (int k = 0; k < 4; ++k) {
      f0[k] = plo(wov[2 * k][0], wov[2 * k + 1][0]); f1[k] = phi(wov[2 * k][0], wov[2 * k + 1][0]);
      f2[k] = plo(wov[2 * k][1], wov[2 * k + 1][1]); f3[k] = phi(wov[2 * k][1], wov[2 * k + 1][1]);
    }
    asm volatile("" : "+v"(f0), "+v"(f1), "+v"(f2), "+v"(f3));
    if (!gall2(v, tag)) {
      for (unsigned spins = 0; spins < SPIN_CAP; ++spins) {
        bool all = true;
        if (!gok2(v[8], tag)) { all = false; v[8] = get2(wsr, ss); }
#pragma unroll
        for (int k = 0; k < 8; ++k) { if (!gok2(v[k], tag)) { all = false; v[k] = get2(wsr, so + k * (H2B * 8)); } }
        if (all) break;
      }
    }
    STAMP(5);
    const float mm = __uint_as_float(v[8][0]);
    float M = sw16_max(row16_max(mm));
    const float wg = __expf(mm - M);
    if (lane < 32) L.wgt[wave][lane] = wg;
    const float Ls = sw16_sum(row16_sum(wg * __uint_as_float(v[8][2])));
    lds_fence();
    float o0 = 0.f, o1 = 0.f;
#pragma unroll
    for (int k4 = 0; k4 < 2; ++k4) {
      const f32x4 wv = *reinterpret_cast<const f32x4*>(&L.wgt[wave][(l15 >> 2) * 8 + k4 * 4]);
#pragma unroll
      for (int k = 0; k < 4; ++k) { o0 = fmaf(wv[k], __uint_as_float(v[k4 * 4 + k][0]), o0); o1 = fmaf(wv[k], __uint_as_float(v[k4 * 4 + k][2]), o1); }
    }
    o0 += dpp<ROR4>(o0); o0 += dpp<ROR8>(o0); o1 += dpp<ROR4>(o1); o1 += dpp<ROR8>(o1);
    const float rl = __builtin_amdgcn_rcpf(Ls);
    const u32x4 oa = quad_gather(pk2(o0 * rl, o1 * rl));
    const f32x4 c0 = mfma(oa, f0, zero4), c1 = mfma(oa, f1, zero4), c2 = mfma(oa, f2, zero4), c3 = mfma(oa, f3, zero4);
    if (lane < 16) *reinterpret_cast<f32x4*>(&L.wpart[wave][lane * 4]) = f32x4{c0[0], c1[0], c2[0], c3[0]};
    bar();
    if (tid < 64) {
      float t = 0.f;
#pragma unroll
      for (int k = 0; k < 8; ++k) t += L.wpart[k][tid];
      put(h3 + (size_t)rb * 512 + cb * 64 + tid, t, tag);
    }
    STAMP(6);
    return;
  }
  if (mb_ >= NMLP + NFIN) return;
  if (mb_ >= NMLP) {
    const int fb = mb_ - NMLP;
    asm volatile("" :: "s"(b2), "s"(out));
    const unsigned xr = *reinterpret_cast<const unsigned*>(x + fb * 64 + (lane & 31) * 2);
    const unsigned short b2r = b2[fb * 64 + lane];
    if (wave == 0) {
      const unsigned src = (unsigned)WS_H3 + ((lane >> 5) * 8 * 512 + fb * 64 + (lane & 31) * 2) * 8;
      u32x4 v[8];
      for (unsigned spins = 0; spins < SPIN_CAP; ++spins) {
#pragma unroll
        for (int k = 0; k < 8; ++k) v[k] = get2(wsr, src + k * 4096);
        if (gall2(v, tag)) break;
        __builtin_amdgcn_s_sleep(1);
      }
      float t0 = 0.f, t1 = 0.f;
#pragma unroll
      for (int k = 0; k < 8; ++k) { t0 += __uint_as_float(v[k][0]); t1 += __uint_as_float(v[k][2]); }
      t0 = sw32_sum(t0); t1 = sw32_sum(t1);
      if (lane < 32) *reinterpret_cast<f32x2*>(&L.hs[lane * 2]) = f32x2{bf16r(bflo(xr) + bf16r(t0)), bf16r(bfhi(xr) + bf16r(t1))};
    }
    {
      u32x4 v[4];
      const int p0 = wave * 7 + (lane >> 5) * 4;
      const unsigned base = (unsigned)WS_H4 + (p0 * 512 + fb * 64 + (lane & 31) * 2) * 8;
      const unsigned last = (lane >> 5) ? 2u * 4096u : 3u * 4096u;
      for (unsigned spins = 0; spins < SPIN_CAP; ++spins) {
#pragma unroll
        for (int k = 0; k < 3; ++k) v[k] = get2(wsr, base + k * 4096);
        v[3] = get2(wsr, base + last);
        if (gall2(v, tag)) break;
        __builtin_amdgcn_s_sleep(1);
      }
      const float w3 = (lane >> 5) ? 0.f : 1.f;
      float t0 = (__uint_as_float(v[0][0]) + __uint_as_float(v[1][0])) + (__uint_as_float(v[2][0]) + w3 * __uint_as_float(v[3][0]));
      float t1 = (__uint_as_float(v[0][2]) + __uint_as_float(v[1][2])) + (__uint_as_float(v[2][2]) + w3 * __uint_as_float(v[3][2]));
      t0 = sw32_sum(t0); t1 = sw32_sum(t1);
      if (lane < 32) *reinterpret_cast<f32x2*>(&L.fpart[wave][lane * 2]) = f32x2{t0, t1};
    }
    STAMP(9);
    bar();
    if (tid < 64) {
      const int slot = (tid & 3) * 16 + (tid >> 2);
      float t = 0.f;
#pragma unroll
      for (int k = 0; k < 8; ++k) t += L.fpart[k][slot];
      const float y = bf16r(bf16r(t) + bflo(b2r));
      const float r = bf16r(L.hs[tid] + y);
      out[fb * 64 + tid] = (unsigned short)(__float_as_uint(r) >> 16);
      if (fb == 0 && tid == 0) *reinterpret_cast<unsigned*>(ws + WS_EPOCH) = tag;
    }
    STAMP(10);
    return;
  }

  PIN();
  {
#pragma unroll
    for (int j = 0; j < 16; ++j)
      w1v[j] = ldw<unsigned>(w1 + (size_t)(wave * 64 + (j >> 3) * 32 + lq * 8 + (j & 7)) * FF + mb_ * 32 + l15 * 2);
#pragma unroll
    for (int j = 0; j < 8; ++j)
      w2v[j] = ldw<u32x2>(w2 + (size_t)(mb_ * 32 + lq * 8 + j) * DM + wave * 64 + l15 * 4);
  }
  const int cp = ((l15 >> 2) & 1) * 16 + lq * 4 + (l15 & 3);
  const unsigned xr = *reinterpret_cast<const unsigned*>(x + wave * 64 + cp * 2);
  const unsigned b1r = *reinterpret_cast<const unsigned*>(b1 + mb_ * 32 + lq * 8 + (l15 & 3) * 2);
  PIN();
  u32x4 e0[2], e1[2], f0, f1, f2, f3;
#pragma unroll
  for (int ks = 0; ks < 2; ++ks)
#pragma unroll
    for (int k = 0; k < 4; ++k) { e0[ks][k] = plo(w1v[ks * 8 + 2 * k], w1v[ks * 8 + 2 * k + 1]); e1[ks][k] = phi(w1v[ks * 8 + 2 * k], w1v[ks * 8 + 2 * k + 1]); }
#pragma unroll
  for (int k = 0; k < 4; ++k) {
    f0[k] = plo(w2v[2 * k][0], w2v[2 * k + 1][0]); f1[k] = phi(w2v[2 * k][0], w2v[2 * k + 1][0]);
    f2[k] = plo(w2v[2 * k][1], w2v[2 * k + 1][1]); f3[k] = phi(w2v[2 * k][1], w2v[2 * k + 1][1]);
  }
  PIN();
  asm volatile("" : "+v"(e0[0]), "+v"(e0[1]), "+v"(e1[0]), "+v"(e1[1]), "+v"(f0), "+v"(f1), "+v"(f2), "+v"(f3));
  float hv0, hv1;
  {
    const unsigned src = (unsigned)WS_H3 + ((l15 >> 3) * 8 * 512 + wave * 64 + cp * 2) * 8;
    u32x4 v[8];
    for (unsigned spins = 0; spins < SPIN_CAP; ++spins) {
#pragma unroll
      for (int k = 0; k < 8; ++k) v[k] = get2(wsr, src + k * 4096);
      if (gall2(v, tag)) break;
      __builtin_amdgcn_s_sleep(1);
    }
    float t0 = 0.f, t1 = 0.f;
#pragma unroll
    for (int k = 0; k < 8; ++k) { t0 += __uint_as_float(v[k][0]); t1 += __uint_as_float(v[k][2]); }
    t0 += dpp<ROR8>(t0); t1 += dpp<ROR8>(t1);
    hv0 = bf16r(bflo(xr) + bf16r(t0));
    hv1 = bf16r(bfhi(xr) + bf16r(t1));
    const float a1 = wave_sum(hv0 + hv1), a2 = wave_sum(hv0 * hv0 + hv1 * hv1);
    if (lane == 0) *reinterpret_cast<f32x2*>(&L.hstat[wave][0]) = f32x2{a1, a2};
  }
  STAMP(7);
  bar();
  {
    float t1 = 0.f, t2 = 0.f;
#pragma unroll
    for (int k = 0; k < 4; ++k) { const f32x4 t = *reinterpret_cast<const f32x4*>(&L.hstat[2 * k][0]); t1 += t[0] + t[2]; t2 += t[1] + t[3]; }
    const float mean2 = t1 * (0.5f / DM);
    const float var2 = fmaxf(t2 * (0.5f / DM) - mean2 * mean2, 0.f);
    const float mb2 = bf16r(mean2), rden2 = __builtin_amdgcn_rcpf(bf16r(__builtin_amdgcn_sqrtf(bf16r(bf16r(var2) + 1e-5f))));
    const u32x4 ha = quad_gather(pk2(bf16r(hv0 - mb2) * rden2, bf16r(hv1 - mb2) * rden2));
    const f32x4 c0a = mfma(ha, e0[0], zero4), c1a = mfma(ha, e1[0], zero4), c0b = mfma(ha, e0[1], zero4), c1b = mfma(ha, e1[1], zero4);
    const float u0 = sw16_sum(lane < 16 ? c0a[0] : c0b[0]), u1 = sw16_sum(lane < 16 ? c1a[0] : c1b[0]);
    if (lane < 16) *reinterpret_cast<f32x2*>(&L.upart[wave][lane * 2]) = f32x2{u0, u1};
  }
  bar();
  {
    float u0 = 0.f, u1 = 0.f;
#pragma unroll
    for (int k = 0; k < 8; ++k) { const f32x2 t = *reinterpret_cast<const f32x2*>(&L.upart[k][lq * 8 + (l15 & 3) * 2]); u0 += t[0]; u1 += t[1]; }
    u0 = bf16r(bf16r(u0) + bflo(b1r)); u1 = bf16r(bf16r(u1) + bfhi(b1r));
    const float z0 = 0.7978845608028654f * (u0 + 0.044715f * u0 * u0 * u0), z1 = 0.7978845608028654f * (u1 + 0.044715f * u1 * u1 * u1);
    const float th0 = 1.f - 2.f * __builtin_amdgcn_rcpf(__expf(2.f * z0) + 1.f), th1 = 1.f - 2.f * __builtin_amdgcn_rcpf(__expf(2.f * z1) + 1.f);
    const u32x4 ga = quad_gather(pk2(0.5f * u0 * (1.f + th0), 0.5f * u1 * (1.f + th1)));
    const f32x4 c0 = mfma(ga, f0, zero4), c1 = mfma(ga, f1, zero4), c2 = mfma(ga, f2, zero4), c3 = mfma(ga, f3, zero4);
    if (lane < 16) {
      u64* dst = h4 + (size_t)mb_ * 512 + wave * 64 + lane;
      put(dst, c0[0], tag); put(dst + 16, c1[0], tag); put(dst + 32, c2[0], tag); put(dst + 48, c3[0], tag);
    }
  }
  STAMP(8);
}

extern "C" void kernel_launch(void* const* d_in, const int* in_sizes, int n_in,
                              void* d_out, int out_size, void* d_ws, size_t ws_size,
                              hipStream_t stream) {
  const unsigned short* x = (const unsigned short*)d_in[0];
  const unsigned short* kc = (const unsigned short*)d_in[1];
  const unsigned short* vc = (const unsigned short*)d_in[2];
  const unsigned short* wq = (const unsigned short*)d_in[3];
  const unsigned short* wo = (const unsigned short*)d_in[4];
  const unsigned short* w1 = (const unsigned short*)d_in[6];
  const unsigned short* b1 = (const unsigned short*)d_in[7];
  const unsigned short* w2 = (const unsigned short*)d_in[8];
  const unsigned short* b2 = (const unsigned short*)d_in[9];
  fused_decode_kernel<<<NBLK, NTHR, 0, stream>>>(x, kc, vc, wq, (unsigned char*)d_ws, wo, w1, w2, b1, b2,
                                                 (unsigned short*)d_out);
}
```
